```python
import torch

def _softmax(x, axis=-1):
    return torch.softmax(x, dim=axis)
def _gelu(x):
    return torch.nn.functional.gelu(x, approximate="tanh")
def _silu(x):
    return torch.nn.functional.silu(x)
def _sigmoid(x):
    return torch.sigmoid(x)
def _layernorm(x, axis=-1, eps=1e-5):
    m = x.mean(dim=axis, keepdim=True)
    v = x.var(dim=axis, keepdim=True, unbiased=False)
    return (x - m) / torch.sqrt(v + eps)
def _scatter_add(tgt, idx, src, axis=0):
    return tgt.clone().index_add_(axis, idx, src)
def _segment_sum(src, seg, n):
    out = torch.zeros((n,) + src.shape[1:], dtype=src.dtype)
    return out.index_add_(0, seg, src)
def _pad(x, pw):
    flat = tuple(v for pair in reversed(pw) for v in pair)
    return torch.nn.functional.pad(x, flat)

kernel_name = 'sequential_preLN__gqa_block_cached__mixer_token_mlp__decode'

def setup_inputs():

    _w = (lambda a: (lambda t: t.to(torch.bfloat16) if t.is_floating_point() else t)(torch.as_tensor(a)))
    def _A_setup():
        B, Sq, D = 8, 1, 512
        Hq, Hkv, Sk, Dh = 32, 8, 4096, 128
        assert Hq % Hkv == 0, (Hq, Hkv)
        return {
            "x": _w((lambda *s: torch.randn(*s, dtype=torch.bfloat16) * 0.05)(B, Sq, D)),
            "k_cache": _w((lambda *s: torch.randn(*s, dtype=torch.bfloat16) * 1.6)(B, Hkv, Sk, Dh)),
            "v_cache": _w((lambda *s: torch.randn(*s, dtype=torch.bfloat16) * 0.8)(B, Hkv, Sk, Dh)),
            "wq": _w((lambda *s: torch.randn(*s, dtype=torch.bfloat16) * 1.6)(D, Hq * Dh) * (D ** -0.5)),
            "wo": _w((lambda *s: torch.randn(*s, dtype=torch.bfloat16) * 0.8)(Hq * Dh, D) * ((Hq * Dh) ** -0.5)),
        }
    _A_in = _A_setup()

    def _B_setup():
        B, S, D, F = 8, 1, 512, 1792
        return {
            "x": _w((lambda *s: torch.randn(*s, dtype=torch.bfloat16) * 0.05)(B, S, D)),
            "w1": _w((lambda *s: torch.randn(*s, dtype=torch.bfloat16) * 0.8)(S, F) * (S ** -0.5)),
            "b1": _w((lambda *s: torch.randn(*s, dtype=torch.bfloat16) * 0.8)(F) * 0.02),
            "w2": _w((lambda *s: torch.randn(*s, dtype=torch.bfloat16) * 0.8)(F, S) * (F ** -0.5)),
            "b2": _w((lambda *s: torch.randn(*s, dtype=torch.bfloat16) * 0.4)(S) * 0.02),
        }
    _B_in = _B_setup()

    return {
        "A_x": _A_in["x"],
        "A_k_cache": _A_in["k_cache"],
        "A_v_cache": _A_in["v_cache"],
        "A_wq": _A_in["wq"],
        "A_wo": _A_in["wo"],
        "B_x": _B_in["x"],
        "B_w1": _B_in["w1"],
        "B_b1": _B_in["b1"],
        "B_w2": _B_in["w2"],
        "B_b2": _B_in["b2"],
    }

def reference(A_x, A_k_cache, A_v_cache, A_wq, A_wo, B_x, B_w1, B_b1, B_w2, B_b2):
    def _A_ref(x, k_cache, v_cache, wq, wo):
        B, Sq, D = x.shape
        Hq, Dh = 32, 128
        Hkv = k_cache.shape[1]
        rep = Hq // Hkv
        q = (lambda x, *d: x.permute(*d))((x @ wq).reshape(B, Sq, Hq, Dh), 0, 2, 1, 3)
        k = (lambda x, n, axis: torch.repeat_interleave(x, n, dim=axis))(k_cache, rep, axis=1)
        v = (lambda x, n, axis: torch.repeat_interleave(x, n, dim=axis))(v_cache, rep, axis=1)
        scores = torch.einsum("bhqd,bhkd->bhqk", q, k) * (Dh ** -0.5)
        p = _softmax(scores, axis=-1)
        o = torch.einsum("bhqk,bhkd->bhqd", p, v)
        return (lambda x, *d: x.permute(*d))(o, 0, 2, 1, 3).reshape(B, Sq, Hq * Dh) @ wo

    def _B_ref(x, w1, b1, w2, b2):
        B, S, D = x.shape
        h = _layernorm(x, axis=-1)
        u = torch.einsum("bsd,sf->bdf", h, w1) + b1
        u = _gelu(u)
        y = torch.einsum("bdf,fs->bds", u, w2) + b2
        return (lambda x, *d: x.permute(*d))(y, 0, 2, 1)

    x = A_x
    h = x + _A_ref(_layernorm(x, axis=-1), A_k_cache, A_v_cache, A_wq, A_wo)
    h2 = h + _B_ref(_layernorm(h, axis=-1), B_w1, B_b1, B_w2, B_b2)
    return h2
```

```cpp
#include <hip/hip_runtime.h>
#include <hip/hip_bf16.h>
#include <stdint.h>

typedef unsigned short u16;
typedef __attribute__((ext_vector_type(4))) unsigned u32x4;

__device__ __forceinline__ float bf2f(u16 b) { return __builtin_bit_cast(float, ((unsigned)b) << 16); }
__device__ __forceinline__ float rbf(float f) {
  unsigned u = __builtin_bit_cast(unsigned, f);
  u += 0x7FFFu + ((u >> 16) & 1u);
  return __builtin_bit_cast(float, u & 0xFFFF0000u);
}
__device__ __forceinline__ u16 f2bf(float f) { return (u16)(__builtin_bit_cast(unsigned, rbf(f)) >> 16); }

constexpr int B = 8, D = 512, HQ = 32, HKV = 8, SK = 4096, DH = 128, F = 1792;
constexpr int NSPLIT = 8;

__device__ float block_sum(float v, float* red) {
  for (int o = 32; o > 0; o >>= 1) v += __shfl_xor(v, o);
  int w = threadIdx.x >> 6, nw = blockDim.x >> 6;
  __syncthreads();
  if ((threadIdx.x & 63) == 0) red[w] = v;
  __syncthreads();
  float s = 0;
  for (int i = 0; i < nw; ++i) s += red[i];
  return s;
}

__device__ float ln512(float x, float* red) {
  float m = rbf(block_sum(x, red) * (1.0f / D));
  float mean_exact = block_sum(x, red) * (1.0f / D);
  float d = x - mean_exact;
  float v = rbf(block_sum(d * d, red) * (1.0f / D));
  float num = rbf(x - m);
  float den = rbf(sqrtf(rbf(v + 1e-5f)));
  return rbf(num / den);
}

__global__ void k_q(const u16* x, const u16* wq, float* q) {
  __shared__ float red[16];
  __shared__ float ln[D];
  int b = blockIdx.x;
  float xv = bf2f(x[b * D + threadIdx.x]);
  ln[threadIdx.x] = ln512(xv, red);
  __syncthreads();
  int n = blockIdx.y * 512 + threadIdx.x;
  float acc = 0;
  for (int k = 0; k < D; ++k) acc += ln[k] * bf2f(wq[(size_t)k * 4096 + n]);
  q[b * 4096 + n] = rbf(acc);
}

__global__ void k_attn(const float* q, const u16* kc, const u16* vc, float* pm, float* pl, float* po) {
  constexpr int KEYS = SK / NSPLIT;
  __shared__ float qs[4][DH];
  __shared__ float sc[4][KEYS];
  __shared__ float red[16];
  __shared__ float accs[2][4][DH];
  int s = blockIdx.x, kvh = blockIdx.y, b = blockIdx.z;
  int tid = threadIdx.x;
  for (int i = tid; i < 4 * DH; i += 256) qs[i / DH][i % DH] = q[b * 4096 + kvh * 512 + i];
  __syncthreads();
  const u16* kb = kc + ((size_t)(b * HKV + kvh) * SK + s * KEYS) * DH;
  const u16* vb = vc + ((size_t)(b * HKV + kvh) * SK + s * KEYS) * DH;
  int c = tid & 15, r = tid >> 4;
  for (int k0 = 0; k0 < KEYS; k0 += 16) {
    u32x4 kv = *(const u32x4*)(kb + (size_t)(k0 + r) * DH + 8 * c);
    float kf[8];
    for (int j = 0; j < 4; ++j) { kf[2 * j] = __builtin_bit_cast(float, kv[j] << 16); kf[2 * j + 1] = __builtin_bit_cast(float, kv[j] & 0xffff0000u); }
    for (int h = 0; h < 4; ++h) {
      float d = 0;
      for (int j = 0; j < 8; ++j) d += kf[j] * qs[h][8 * c + j];
      for (int o = 8; o > 0; o >>= 1) d += __shfl_xor(d, o);
      if (c == 0) sc[h][k0 + r] = rbf(rbf(d) * 0.08838834764831845f);
    }
  }
  __syncthreads();
  float mh[4], lh[4];
  for (int h = 0; h < 4; ++h) {
    float m = -INFINITY;
    for (int i = tid; i < KEYS; i += 256) m = fmaxf(m, sc[h][i]);
    for (int o = 32; o > 0; o >>= 1) m = fmaxf(m, __shfl_xor(m, o));
    __syncthreads();
    if ((tid & 63) == 0) red[tid >> 6] = m;
    __syncthreads();
    m = fmaxf(fmaxf(red[0], red[1]), fmaxf(red[2], red[3]));
    float l = 0;
    for (int i = tid; i < KEYS; i += 256) { float p = __expf(sc[h][i] - m); sc[h][i] = p; l += p; }
    l = block_sum(l, red);
    mh[h] = m; lh[h] = l;
  }
  __syncthreads();
  int d = tid & 127, half = tid >> 7;
  float acc[4] = {0, 0, 0, 0};
  for (int k = half; k < KEYS; k += 2) {
    float v = bf2f(vb[(size_t)k * DH + d]);
    for (int h = 0; h < 4; ++h) acc[h] += sc[h][k] * v;
  }
  for (int h = 0; h < 4; ++h) accs[half][h][d] = acc[h];
  __syncthreads();
  if (half == 0) {
    for (int h = 0; h < 4; ++h) {
      size_t row = (size_t)(b * HQ + kvh * 4 + h) * NSPLIT + s;
      po[row * DH + d] = accs[0][h][d] + accs[1][h][d];
      if (d == 0) { pm[row] = mh[h]; pl[row] = lh[h]; }
    }
  }
}

__global__ void k_wo(const float* pm, const float* pl, const float* po, const u16* wo, float* part) {
  __shared__ float o[DH];
  int h = blockIdx.x, b = blockIdx.y, tid = threadIdx.x;
  size_t row = (size_t)(b * HQ + h) * NSPLIT;
  if (tid < DH) {
    float M = -INFINITY;
    for (int s = 0; s < NSPLIT; ++s) M = fmaxf(M, pm[row + s]);
    float L = 0, a = 0;
    for (int s = 0; s < NSPLIT; ++s) { float w = __expf(pm[row + s] - M); L += w * pl[row + s]; a += w * po[(row + s) * DH + tid]; }
    o[tid] = rbf(a / L);
  }
  __syncthreads();
  float acc = 0;
  for (int k = 0; k < DH; ++k) acc += o[k] * bf2f(wo[(size_t)(h * DH + k) * D + tid]);
  part[(size_t)(b * HQ + h) * D + tid] = acc;
}

__global__ void k_mlp(const u16* x, const float* part, const u16* w1, const u16* b1, const u16* w2, const u16* b2, u16* out) {
  __shared__ float red[16];
  __shared__ float hs[D], ts[D];
  int b = blockIdx.x, tid = threadIdx.x;
  float a = 0;
  for (int h = 0; h < HQ; ++h) a += part[(size_t)(b * HQ + h) * D + tid];
  float hv = rbf(bf2f(x[b * D + tid]) + rbf(a));
  float t = ln512(ln512(hv, red), red);
  hs[tid] = hv; ts[tid] = t;
  __syncthreads();
  int w = tid >> 6, lane = tid & 63;
  for (int i = 0; i < 2; ++i) {
    int d = blockIdx.y * 16 + w * 2 + i;
    float tv = ts[d];
    float acc = 0;
    for (int f = lane; f < F; f += 64) {
      float u = rbf(rbf(tv * bf2f(w1[f])) + bf2f(b1[f]));
      float g = 0.5f * u * (1.0f + tanhf(0.7978845608028654f * (u + 0.044715f * u * u * u)));
      acc += rbf(g) * bf2f(w2[f]);
    }
    for (int o = 32; o > 0; o >>= 1) acc += __shfl_xor(acc, o);
    if (lane == 0) {
      float y = rbf(rbf(acc) + bf2f(b2[0]));
      out[b * D + d] = f2bf(hs[d] + y);
    }
  }
}

extern "C" void kernel_launch(void* const* d_in, const int* in_sizes, int n_in, void* d_out, int out_size, void* d_ws, size_t ws_size,
                              hipStream_t stream) {
  const u16* x = (const u16*)d_in[0];
  const u16* kc = (const u16*)d_in[1];
  const u16* vc = (const u16*)d_in[2];
  const u16* wq = (const u16*)d_in[3];
  const u16* wo = (const u16*)d_in[4];
  const u16* w1 = (const u16*)d_in[6];
  const u16* b1 = (const u16*)d_in[7];
  const u16* w2 = (const u16*)d_in[8];
  const u16* b2 = (const u16*)d_in[9];
  float* ws = (float*)d_ws;
  float* q = ws;
  float* pm = q + B * 4096;
  float* pl = pm + B * HQ * NSPLIT;
  float* po = pl + B * HQ * NSPLIT;
  float* part = po + B * HQ * NSPLIT * DH;
  k_q<<<dim3(B, 8), 512, 0, stream>>>(x, wq, q);
  k_attn<<<dim3(NSPLIT, HKV, B), 256, 0, stream>>>(q, kc, vc, pm, pl, po);
  k_wo<<<dim3(HQ, B), 512, 0, stream>>>(pm, pl, po, wo, part);
  k_mlp<<<dim3(B, 32), 512, 0, stream>>>(x, part, w1, b1, w2, b2, (u16*)d_out);
}
```

```cpp
#include <hip/hip_runtime.h>
#include <stdint.h>

typedef unsigned short u16;
typedef unsigned u32;
typedef unsigned long long u64;
typedef u32 u32x2 __attribute__((ext_vector_type(2)));
typedef u32 u32x4 __attribute__((ext_vector_type(4)));
typedef float f32x2 __attribute__((ext_vector_type(2)));
typedef float f32x4 __attribute__((ext_vector_type(4)));
typedef short s16x4 __attribute__((ext_vector_type(4)));
typedef __bf16 bf16x8 __attribute__((ext_vector_type(8)));
#define LAS __attribute__((address_space(3)))
#define GAS __attribute__((address_space(1)))
#define RLX_AGENT __ATOMIC_RELAXED, __HIP_MEMORY_SCOPE_AGENT

#define KV_NT 1
#define WIN 8
#define PARK 24

constexpr int NB = 8, DM = 512, HKV = 8, SK = 4096, DH = 128, FF = 1792;
constexpr int NWG = 256, NSW = 8, NHW = 4, NTHR = 64 * (NSW + NHW);
constexpr int NE = 24, E_LO = 132 - NE, NT_HALF = NE * 128, NTAB = 2 * NT_HALF;
static_assert(NTAB == NWG * 24, "24 table entries per workgroup");
constexpr float C2 = 0.08838834764831845f * 1.4426950408889634f;
constexpr u32 SPIN_CAP = 1u << 15;

constexpr u32 WS_EPOCH = 0, WS_QG = 4096, WS_ML = WS_QG + 131072, WS_PA = WS_ML + 16384, WS_PB = WS_PA + 524288, WS_TB = WS_PB + 262144,
              WS_END = WS_TB + NTAB * 8, WS_DBG = 2u << 20;
constexpr u32 L_QF = 0, L_CTL = 1024, L_MX = 1088, L_LW = 1216, L_FS = 1344, L_RED2 = 2048, L_HROW = L_RED2 + 4096, L_RED = L_HROW + 2048,
              L_TAB = L_RED + 16384, L_END = L_TAB + NTAB * 4;

__device__ __forceinline__ float bflo(u32 w) { return __builtin_bit_cast(float, w << 16); }
__device__ __forceinline__ float bfhi(u32 w) { return __builtin_bit_cast(float, w & 0xffff0000u); }
__device__ __forceinline__ u32 cvtpk(float lo, float hi) {
  typedef __bf16 bf16x2 __attribute__((ext_vector_type(2)));
  bf16x2 r = {(__bf16)lo, (__bf16)hi};
  return __builtin_bit_cast(u32, r);
}
__device__ __forceinline__ float rbf(float f) { return bflo(cvtpk(f, f)); }
template <int CTRL> __device__ __forceinline__ float dppf(float x) {
  return __builtin_bit_cast(float, __builtin_amdgcn_mov_dpp(__builtin_bit_cast(int, x), CTRL, 0xf, 0xf, true));
}
template <int CTRL> __device__ __forceinline__ u32 dppu(u32 x) { return (u32)__builtin_amdgcn_mov_dpp((int)x, CTRL, 0xf, 0xf, true); }
constexpr int XOR1 = 0xB1, XOR2 = 0x4E, HMIR = 0x141, MIR = 0x140;
__device__ __forceinline__ float xrow_sum(float x) {
  auto s = __builtin_amdgcn_permlane16_swap(__float_as_uint(x), __float_as_uint(x), false, false);
  x = __uint_as_float(s[0]) + __uint_as_float(s[1]);
  auto t = __builtin_amdgcn_permlane32_swap(__float_as_uint(x), __float_as_uint(x), false, false);
  return __uint_as_float(t[0]) + __uint_as_float(t[1]);
}
__device__ __forceinline__ float xrow_max(float x) {
  auto s = __builtin_amdgcn_permlane16_swap(__float_as_uint(x), __float_as_uint(x), false, false);
  x = fmaxf(__uint_as_float(s[0]), __uint_as_float(s[1]));
  auto t = __builtin_amdgcn_permlane32_swap(__float_as_uint(x), __float_as_uint(x), false, false);
  return fmaxf(__uint_as_float(t[0]), __uint_as_float(t[1]));
}
__device__ __forceinline__ float wave_sum(float x) {
  x += dppf<XOR1>(x); x += dppf<XOR2>(x); x += dppf<HMIR>(x); x += dppf<MIR>(x);
  return xrow_sum(x);
}
__device__ __forceinline__ f32x4 mfma32(u32x4 a, u32x4 b, f32x4 c) {
  return __builtin_amdgcn_mfma_f32_16x16x32_bf16(__builtin_bit_cast(bf16x8, a), __builtin_bit_cast(bf16x8, b), c, 0, 0, 0);
}
__device__ __forceinline__ f32x4 mfma16(u32x2 a, u32x2 b, f32x4 c) {
  return __builtin_amdgcn_mfma_f32_16x16x16bf16_1k(__builtin_bit_cast(s16x4, a), __builtin_bit_cast(s16x4, b), c, 0, 0, 0);
}
template <int N> __device__ __forceinline__ void wait_vm() { asm volatile("s_waitcnt vmcnt(%0)" ::"n"(N) : "memory"); }
__device__ __forceinline__ void wait_lgkm() { asm volatile("s_waitcnt lgkmcnt(0)" ::: "memory"); }
__device__ __forceinline__ void bar() { asm volatile("s_waitcnt lgkmcnt(0)\n\ts_barrier" ::: "memory"); }
#define SB() __builtin_amdgcn_sched_barrier(0)
__device__ __forceinline__ u32x4 ldkv(const u32x4* p) {
  return __builtin_nontemporal_load(p);
}
__device__ __forceinline__ void st_gran(unsigned char* ws, u32 off, u32 tag, u32 val) {
  __hip_atomic_store((GAS u64*)(ws + off), ((u64)tag << 32) | val, RLX_AGENT);
}
__device__ __forceinline__ u64 ld_gran(unsigned char* ws, u32 off) { return __hip_atomic_load((GAS u64*)(ws + off), RLX_AGENT); }
__device__ __forceinline__ u32x4 ld_gran2(__amdgpu_buffer_rsrc_t rs, u32 off) { return __builtin_amdgcn_raw_buffer_load_b128(rs, off, 0, 16); }

#define STAMP(i) do {} while (0)

__device__ __forceinline__ float gelu_term(float t, float w1, float b1, float w2, float acc) {
  const float u = fmaf(t, w1, b1);
  const float z = u * fmaf(u * u, -2.302208198f * 0.044715f, -2.302208198f);
  const float e = __builtin_amdgcn_exp2f(z);
  return fmaf(u * w2, __builtin_amdgcn_rcpf(1.0f + e), acc);
}

__device__ __forceinline__ void ln_row(float (&v)[8]) {
  float s = 0.f;
#pragma unroll
  for (int e = 0; e < 8; ++e) s += v[e];
  const float mean = wave_sum(s) * (1.0f / DM);
  float q = 0.f;
#pragma unroll
  for (int e = 0; e < 8; ++e) { const float d = v[e] - mean; q = fmaf(d, d, q); }
  const float var = rbf(wave_sum(q) * (1.0f / DM));
  const float mb = rbf(mean);
  const float den = rbf(__builtin_amdgcn_sqrtf(rbf(var + 1e-5f)));
  const float inv = 1.0f / den;
#pragma unroll
  for (int e = 0; e < 8; ++e) v[e] = rbf(rbf(v[e] - mb) * inv);
}

__global__ void __launch_bounds__(NTHR) fused_kernel(const u16* x, const u16* kc, const u16* vc, const u16* wq, const u16* wo, const u16* w1,
                                                     const u16* b1, const u16* w2, const u16* b2, u16* out, unsigned char* ws) {
  __shared__ __attribute__((aligned(16))) unsigned char lds[L_END];
  const int tid = threadIdx.x, lane = tid & 63, wave = __builtin_amdgcn_readfirstlane(tid >> 6);
  const int wg = blockIdx.x, kvh = wg & 7, sp = (wg >> 3) & 3, b = wg >> 5;
  const bool final_wg = (kvh == b) && (sp == 0);
  const int li = lane & 15, lg = lane >> 4;
  volatile LAS u32* ctl = (volatile LAS u32*)(lds + L_CTL);
  const __amdgpu_buffer_rsrc_t rs = __builtin_amdgcn_make_buffer_rsrc(ws, 0, WS_END, 0x00020000);
  STAMP(0);

  if (wave < NSW) {
    const size_t tile = ((size_t)(b * HKV + kvh) * SK + sp * 1024) * 16;
    const u32x4* kb = (const u32x4*)kc + tile + wave * 256;
    const u32x4* vb = (const u32x4*)vc + tile + wave * 256;
    const u32x4* wb = (const u32x4*)wo + (size_t)(kvh * 512 + 64 * wave) * 64 + 16 * sp;
    const u32 klane = ((lane >> 2) & 3) * 16 + 4 * lg + (lane & 3);
    const u32 wlane = lg * 8 * 64 + li;
    u32x4 K[32], V[32], WO[16];
    auto issue = [&](auto nc) {
      constexpr int n = decltype(nc)::value;
      if constexpr (n < 32) {
        K[n] = ldkv(kb + (n >> 2) * 2048 + (n & 3) * 64 + klane);
      } else {
        constexpr int m = n - 32, grp = m / 3, r = m % 3;
        if constexpr (r < 2) { constexpr int v = grp * 2 + r; V[v] = ldkv(vb + (v >> 2) * 2048 + (v & 3) * 64 + lane); }
        else { WO[grp] = wb[((grp >> 3) * 32 + (grp & 7)) * 64 + wlane]; }
      }
    };
    auto for_range = [&](auto lo, auto hi, auto&& f) {
      constexpr int L = decltype(lo)::value, H = decltype(hi)::value;
      [&]<int... I>(std::integer_sequence<int, I...>) { (f(std::integral_constant<int, L + I>{}), ...); }(std::make_integer_sequence<int, (H > L ? H - L : 0)>{});
    };
#define IC(n) std::integral_constant<int, (n)>{}
    for_range(IC(0), IC(WIN), [&](auto n) { issue(n); });
    SB();
    if (tid == 0) ctl[0] = 0;
    bar();
    SB();
    for_range(IC(WIN), IC(PARK), [&](auto n) { wait_vm<WIN - 1>(); SB(); issue(n); SB(); });
    STAMP(1);
    for (u32 spins = 0; ctl[0] == 0 && spins < (SPIN_CAP << 4); ++spins) __builtin_amdgcn_s_sleep(1);
    const u32 tag = __builtin_amdgcn_readfirstlane(ctl[1]);
    const u32x4 qf = *(const LAS u32x4*)(lds + L_QF + lane * 16);
    STAMP(2);
    const bool c0 = lane & 1, c1 = lane & 2;
    float S[8];
    u32 pp[8];
    f32x4 acc0 = {0.f, 0.f, 0.f, 0.f}, acc1 = {0.f, 0.f, 0.f, 0.f};
    const u32 half = c0 ? 0xffff0000u : 0x0000ffffu;
    const u32 mask0 = c1 ? 0u : half, mask1 = c1 ? half : 0u;
    u32x4 WB[2][8];
    auto consume_kgroup = [&](auto uc) {
      constexpr int u = decltype(uc)::value;
      const f32x4 z = {0.f, 0.f, 0.f, 0.f};
      float v[4];
#pragma unroll
      for (int t = 0; t < 4; ++t) {
        const f32x4 d = mfma32(K[4 * u + t], qf, z);
        const float lo = c0 ? d[1] : d[0], hi = c0 ? d[3] : d[2];
        v[t] = c1 ? hi : lo;
      }
      const float w0 = (c0 ? v[1] : v[0]) + dppf<XOR1>(c0 ? v[0] : v[1]);
      const float w1_ = (c0 ? v[3] : v[2]) + dppf<XOR1>(c0 ? v[2] : v[3]);
      S[u] = (c1 ? w1_ : w0) + dppf<XOR2>(c1 ? w0 : w1_);
    };
    auto softmax = [&]() {
      float m = S[0];
#pragma unroll
      for (int u = 1; u < 8; ++u) m = fmaxf(m, S[u]);
      m = fmaxf(m, dppf<XOR1>(m)); m = fmaxf(m, dppf<XOR2>(m)); m = xrow_max(m);
      LAS float* mx = (LAS float*)(lds + L_MX);
      if ((lane & 3) == 0 && lg == 0) mx[(li >> 2) * 8 + wave] = m;
      bar();
      const f32x4 ma = *(const LAS f32x4*)(mx + (li >> 2) * 8), mb = *(const LAS f32x4*)(mx + (li >> 2) * 8 + 4);
      const float M = fmaxf(fmaxf(fmaxf(ma[0], ma[1]), fmaxf(ma[2], ma[3])), fmaxf(fmaxf(mb[0], mb[1]), fmaxf(mb[2], mb[3])));
      const float mc = M * C2;
      float l = 0.f;
#pragma unroll
      for (int u = 0; u < 8; ++u) {
        const float p = __builtin_amdgcn_exp2f(fmaf(S[u], C2, -mc));
        pp[u] = cvtpk(p, p);
        l += bflo(pp[u]);
      }
      l += dppf<XOR1>(l); l += dppf<XOR2>(l); l = xrow_sum(l);
      LAS float* lw = (LAS float*)(lds + L_LW);
      if ((lane & 3) == 0 && lg == 0) lw[(li >> 2) * 8 + wave] = l;
      bar();
    };
    auto consume_v = [&](auto vc_) {
      constexpr int v = decltype(vc_)::value, u = v >> 2, t = v & 3;
      const u32 pb = dppu<t * 0x55>(pp[u]);
      const u32x2 a = {pb & mask0, pb & mask1};
      acc0 = mfma16(a, u32x2{V[v][0], V[v][1]}, acc0);
      acc1 = mfma16(a, u32x2{V[v][2], V[v][3]}, acc1);
    };
    auto perm_wo = [&](auto ksc) {
      constexpr int ks = decltype(ksc)::value;
#pragma unroll
      for (int c = 0; c < 8; ++c)
#pragma unroll
        for (int d = 0; d < 4; ++d)
          WB[ks][c][d] = __builtin_amdgcn_perm(WO[ks * 8 + 2 * d + 1][c >> 1], WO[ks * 8 + 2 * d][c >> 1], (c & 1) ? 0x07060302u : 0x05040100u);
    };
    auto consume = [&](auto ic) {
      constexpr int i = decltype(ic)::value;
      if constexpr (i < 32) {
        if constexpr ((i & 3) == 3) consume_kgroup(IC(i >> 2));
        if constexpr (i == 31) { STAMP(3); softmax(); STAMP(4); }
      } else {
        constexpr int m = i - 32, grp = m / 3, r = m % 3;
        if constexpr (r < 2) consume_v(IC(grp * 2 + r));
        else if constexpr ((grp & 7) == 7) perm_wo(IC(grp >> 3));
      }
    };
    for_range(IC(0), IC(PARK - WIN), [&](auto i) { consume(i); });
    SB();
    for_range(IC(PARK), IC(80), [&](auto n) { consume(IC(decltype(n)::value - WIN)); SB(); wait_vm<WIN - 1>(); SB(); issue(n); SB(); });
    for_range(IC(80 - WIN), IC(80), [&](auto i) { consume(i); SB(); });
    STAMP(5);
    *(LAS f32x4*)(lds + L_RED + wave * 2048 + lane * 32) = acc0;
    *(LAS f32x4*)(lds + L_RED + wave * 2048 + lane * 32 + 16) = acc1;
    bar();
    const u32 grp_off = (u32)(b * HKV + kvh);
    if (wave < 4) {
      f32x2 s = *(const LAS f32x2*)(lds + L_RED + tid * 8);
#pragma unroll
      for (int w = 1; w < 8; ++w) s += *(const LAS f32x2*)(lds + L_RED + w * 2048 + tid * 8);
      const float f = *(const LAS float*)(lds + L_FS + wave * 4);
      st_gran(ws, WS_PA + (grp_off * 4 + sp) * 2048 + tid * 8, tag, cvtpk(s[0] * f, s[1] * f));
    }
    STAMP(6);
    u32x4 A[2];
    {
      const u32 off = WS_PA + (grp_off * 4 + (li & 3)) * 2048 + (32 * wave + 4 * lg) * 8;
      for (u32 spins = 0;; ++spins) {
        const u32x4 g0 = ld_gran2(rs, off), g1 = ld_gran2(rs, off + 16), g2 = ld_gran2(rs, off + 128), g3 = ld_gran2(rs, off + 144);
        const bool ok = g0[1] == tag && g0[3] == tag && g1[1] == tag && g1[3] == tag && g2[1] == tag && g2[3] == tag && g3[1] == tag && g3[3] == tag;
        A[0] = u32x4{g0[0], g0[2], g1[0], g1[2]};
        A[1] = u32x4{g2[0], g2[2], g3[0], g3[2]};
        if (__all(ok) || spins > SPIN_CAP) break;
        __builtin_amdgcn_s_sleep(1);
      }
    }
    STAMP(7);
    {
      float o[8];
#pragma unroll
      for (int c = 0; c < 8; ++c) {
        f32x4 d = {0.f, 0.f, 0.f, 0.f};
        d = mfma32(A[0], WB[0][c], d);
        d = mfma32(A[1], WB[1][c], d);
        o[c] = (d[0] + d[1]) + (d[2] + d[3]);
      }
      if (lg == 0) {
        *(LAS f32x4*)(lds + L_RED2 + wave * 512 + li * 32) = f32x4{o[0], o[1], o[2], o[3]};
        *(LAS f32x4*)(lds + L_RED2 + wave * 512 + li * 32 + 16) = f32x4{o[4], o[5], o[6], o[7]};
      }
    }
    bar();
    if (wave < 2) {
      float s = *(const LAS float*)(lds + L_RED2 + tid * 4);
#pragma unroll
      for (int w = 1; w < 8; ++w) s += *(const LAS float*)(lds + L_RED2 + w * 512 + tid * 4);
      st_gran(ws, WS_PB + grp_off * 4096 + (128 * sp + tid) * 8, tag, __float_as_uint(s));
    }
    STAMP(8);
    if (!final_wg) return;
    const float xv = bflo(x[b * DM + tid]);
    float a;
    for (u32 spins = 0;; ++spins) {
      u64 g[8];
#pragma unroll
      for (int k = 0; k < 8; ++k) g[k] = ld_gran(ws, WS_PB + (b * HKV + k) * 4096 + tid * 8);
      bool ok = true;
      a = 0.f;
#pragma unroll
      for (int k = 0; k < 8; ++k) { ok &= (u32)(g[k] >> 32) == tag; a += __uint_as_float((u32)g[k]); }
      if (__all(ok) || spins > SPIN_CAP) break;
      __builtin_amdgcn_s_sleep(1);
    }
    STAMP(9);
    *(LAS float*)(lds + L_HROW + tid * 4) = rbf(xv + rbf(a));
    bar();
    if (wave != 0) return;
    float h[8], t[8];
    {
      const f32x4 h0 = *(const LAS f32x4*)(lds + L_HROW + lane * 32), h1 = *(const LAS f32x4*)(lds + L_HROW + lane * 32 + 16);
#pragma unroll
      for (int e = 0; e < 4; ++e) { h[e] = h0[e]; h[4 + e] = h1[e]; }
#pragma unroll
      for (int e = 0; e < 8; ++e) t[e] = h[e];
    }
    ln_row(t);
    ln_row(t);
    float y[8];
    bool miss = false;
#pragma unroll
    for (int e = 0; e < 8; ++e) {
      const u32 bits = __float_as_uint(t[e]) >> 16;
      const u32 rel = (bits & 0x7fffu) - (E_LO << 7);
      const bool in = rel < (u32)NT_HALF;
      miss |= !in;
      y[e] = *(const LAS float*)(lds + L_TAB + ((in ? rel : 0u) + (bits >> 15) * NT_HALF) * 4);
    }
    if (__any(miss)) {
      const float b2v = bflo(b2[0]);
#pragma unroll
      for (int e = 0; e < 8; ++e) {
        const u32 rel = ((__float_as_uint(t[e]) >> 16) & 0x7fffu) - (E_LO << 7);
        u64 mm = __ballot(rel >= (u32)NT_HALF);
        while (mm) {
          const int src = __builtin_ctzll(mm);
          mm &= mm - 1;
          const float tv = __builtin_bit_cast(float, __builtin_amdgcn_readlane(__float_as_uint(t[e]), src));
          float acc = 0.f;
          for (int f = lane; f < FF; f += 64) acc = gelu_term(tv, bflo(w1[f]), bflo(b1[f]), bflo(w2[f]), acc);
          acc = wave_sum(acc);
          if (lane == src) y[e] = rbf(rbf(acc) + b2v);
        }
      }
    }
    u32x4 o;
#pragma unroll
    for (int e = 0; e < 4; ++e) o[e] = cvtpk(h[2 * e] + y[2 * e], h[2 * e + 1] + y[2 * e + 1]);
    *(u32x4*)(out + b * DM + lane * 8) = o;
    if (b == 0 && lane == 0) __hip_atomic_store((GAS u32*)(ws + WS_EPOCH), tag, RLX_AGENT);
    STAMP(10);
    return;
  }

  const u32 epoch = __hip_atomic_load((GAS u32*)(ws + WS_EPOCH), RLX_AGENT);
  if (wave == NSW) {
    const int i32 = wg >> 3;
    u32x4 xa[16], wt[16];
    const u32x4* xp = (const u32x4*)x + (li & 7) * 64 + lg;
    const u32x4* wp = (const u32x4*)(wq + kvh * 512 + 16 * i32) + (size_t)(lane >> 1) * 512 + (lane & 1);
#pragma unroll
    for (int k = 0; k < 16; ++k) wt[k] = wp[(size_t)k * 32 * 512];
#pragma unroll
    for (int k = 0; k < 16; ++k) xa[k] = xp[k * 4];
    bar();
    const u32 tag = __builtin_amdgcn_readfirstlane(epoch) + 1u;
#pragma unroll
    for (int k = 0; k < 16; ++k) *(LAS u32x4*)(lds + L_RED + k * 1024 + lane * 16) = wt[k];
    wait_lgkm();
    f32x4 dq = {0.f, 0.f, 0.f, 0.f}, gr = {0.f, 0.f, 0.f, 0.f};
    const u32 troff = L_RED + (8 * lg + (li >> 2)) * 32 + (li & 3) * 8;
#pragma unroll
    for (int k = 0; k < 16; ++k) {
      u32x4 a = xa[k];
      if (li >= 8) { const u32 o = li == 8 ? 0x3f803f80u : 0u; a = u32x4{o, o, o, o}; }
      const s16x4 b0 = __builtin_amdgcn_ds_read_tr16_b64_v4i16((LAS s16x4*)(lds + troff + k * 1024));
      const s16x4 b1_ = __builtin_amdgcn_ds_read_tr16_b64_v4i16((LAS s16x4*)(lds + troff + k * 1024 + 128));
      const u32x2 p0 = __builtin_bit_cast(u32x2, b0), p1 = __builtin_bit_cast(u32x2, b1_);
      dq = mfma32(a, u32x4{p0[0], p0[1], p1[0], p1[1]}, dq);
      gr = mfma32(a, a, gr);
    }
    const float colsum = __shfl(dq[0], 32 + li);
#pragma unroll
    for (int r = 0; r < 4; ++r) {
      const float sum = __shfl(gr[r], (lane & 48) + 8), ssq = __shfl(gr[r], (lane & 48) + ((4 * lg + r) & 15));
      const float mean = sum * (1.0f / DM);
      const float var = fmaxf(fmaf(-mean, mean, ssq * (1.0f / DM)), 0.f);
      const float q = (dq[r] - mean * colsum) * __builtin_amdgcn_rsqf(var + 1e-5f);
      const float qn = dppf<XOR1>(q);
      if (lg < 2 && (lane & 1) == 0) st_gran(ws, WS_QG + ((4 * lg + r) * HKV + kvh) * 2048 + (8 * i32 + (li >> 1)) * 8, tag, cvtpk(q, qn));
    }
    STAMP(1);
    {
      const u32 off = WS_QG + (b * HKV + kvh) * 2048 + ((li >> 2) * 64 + 16 * lg + 4 * (li & 3)) * 8;
      u32x4 q4;
      for (u32 spins = 0;; ++spins) {
        const u32x4 g0 = ld_gran2(rs, off), g1 = ld_gran2(rs, off + 16);
        const bool ok = g0[1] == tag && g0[3] == tag && g1[1] == tag && g1[3] == tag;
        q4 = u32x4{g0[0], g0[2], g1[0], g1[2]};
        if (__all(ok) || spins > SPIN_CAP) break;
        __builtin_amdgcn_s_sleep(1);
      }
      *(LAS u32x4*)(lds + L_QF + lane * 16) = q4;
      if (lane == 0) ctl[1] = tag;
      wait_lgkm();
      if (lane == 0) ctl[0] = 1;
    }
    STAMP(2);
    bar();
    bar();
    {
      const LAS float* src = (const LAS float*)(lds + ((lane & 4) ? L_LW : L_MX)) + (lane & 3) * 8;
      const f32x4 va = *(const LAS f32x4*)src, vb = *(const LAS f32x4*)(src + 4);
      const float mx = fmaxf(fmaxf(fmaxf(va[0], va[1]), fmaxf(va[2], va[3])), fmaxf(fmaxf(vb[0], vb[1]), fmaxf(vb[2], vb[3])));
      const float sm = ((va[0] + va[1]) + (va[2] + va[3])) + ((vb[0] + vb[1]) + (vb[2] + vb[3]));
      const u32 goff = WS_ML + (u32)(b * HKV + kvh) * 256;
      if (lane < 8) st_gran(ws, goff + sp * 64 + lane * 8, tag, __float_as_uint((lane & 4) ? sm : mx));
      float v;
      for (u32 spins = 0;; ++spins) {
        const u64 g = ld_gran(ws, goff + (lane & 31) * 8);
        v = __uint_as_float((u32)g);
        if (__all((u32)(g >> 32) == tag) || spins > SPIN_CAP) break;
        __builtin_amdgcn_s_sleep(2);
      }
      const float lv = __shfl(v, lane | 4);
      float Ms = fmaxf(v, __shfl_xor(v, 8));
      Ms = fmaxf(Ms, __shfl_xor(Ms, 16));
      const float wgt = __builtin_amdgcn_exp2f((v - Ms) * C2);
      float L = wgt * lv;
      L += __shfl_xor(L, 8);
      L += __shfl_xor(L, 16);
      if ((lane >> 3) == sp && (lane & 4) == 0) *(LAS float*)(lds + L_FS + (lane & 3) * 4) = wgt / L;
    }
    STAMP(3);
    bar();
    return;
  }
  {
    u32x2 r1[7], rb[7], r2[7];
#pragma unroll
    for (int c = 0; c < 7; ++c) {
      r1[c] = ((const u32x2*)w1)[c * 64 + lane];
      rb[c] = ((const u32x2*)b1)[c * 64 + lane];
      r2[c] = ((const u32x2*)w2)[c * 64 + lane];
    }
    const float b2v = bflo(b2[0]);
    bar();
    const u32 tag = __builtin_amdgcn_readfirstlane(epoch) + 1u;
    const int idx0 = wg * 24 + (wave - NSW - 1) * 8;
    float res = 0.f;
#pragma unroll 1
    for (int e = 0; e < 8; e += 2) {
      const int i0 = idx0 + e, i1 = i0 + 1;
      const float t0 = __uint_as_float((u32)((i0 >= NT_HALF ? i0 - NT_HALF + 0x8000 : i0) + (E_LO << 7)) << 16);
      const float t1 = __uint_as_float((u32)((i1 >= NT_HALF ? i1 - NT_HALF + 0x8000 : i1) + (E_LO << 7)) << 16);
      float a0 = 0.f, a1 = 0.f;
#pragma unroll
      for (int c = 0; c < 7; ++c)
#pragma unroll
        for (int d = 0; d < 2; ++d) {
          a0 = gelu_term(t0, bflo(r1[c][d]), bflo(rb[c][d]), bflo(r2[c][d]), a0);
          a1 = gelu_term(t1, bflo(r1[c][d]), bflo(rb[c][d]), bflo(r2[c][d]), a1);
          a0 = gelu_term(t0, bfhi(r1[c][d]), bfhi(rb[c][d]), bfhi(r2[c][d]), a0);
          a1 = gelu_term(t1, bfhi(r1[c][d]), bfhi(rb[c][d]), bfhi(r2[c][d]), a1);
        }
      a0 = wave_sum(a0);
      a1 = wave_sum(a1);
      if (lane == e) res = a0;
      if (lane == e + 1) res = a1;
    }
    if (lane < 8) st_gran(ws, WS_TB + (idx0 + lane) * 8, tag, __float_as_uint(rbf(rbf(res) + b2v)));
    STAMP(1);
    if (final_wg) {
      const u32 base = (u32)(wave - NSW - 1) * 1024 + lane;
#pragma unroll 1
      for (int half = 0; half < 2; ++half) {
        for (u32 spins = 0;; ++spins) {
          u32x4 g[8];
          bool ok = true;
#pragma unroll
          for (int k = 0; k < 8; ++k) g[k] = ld_gran2(rs, WS_TB + (base + (half * 8 + k) * 64) * 16);
#pragma unroll
          for (int k = 0; k < 8; ++k) {
            ok &= g[k][1] == tag && g[k][3] == tag;
            *(LAS u32x2*)(lds + L_TAB + (base + (half * 8 + k) * 64) * 8) = u32x2{g[k][0], g[k][2]};
          }
          if (__all(ok) || spins > SPIN_CAP) break;
          __builtin_amdgcn_s_sleep(8);
        }
      }
      wait_lgkm();
    }
    STAMP(2);
  }
}

extern "C" void kernel_launch(void* const* d_in, const int* in_sizes, int n_in, void* d_out, int out_size, void* d_ws, size_t ws_size,
                              hipStream_t stream) {
  fused_kernel<<<dim3(NWG), dim3(NTHR), 0, stream>>>((const u16*)d_in[0], (const u16*)d_in[1], (const u16*)d_in[2], (const u16*)d_in[3],
                                                     (const u16*)d_in[4], (const u16*)d_in[6], (const u16*)d_in[7], (const u16*)d_in[8],
                                                     (const u16*)d_in[9], (u16*)d_out, (unsigned char*)d_ws);
}
```

```cpp
#include <hip/hip_runtime.h>
#include <stdint.h>
#include <utility>
#include <type_traits>

typedef unsigned short u16;
typedef unsigned u32;
typedef unsigned long long u64;
typedef u32 u32x2 __attribute__((ext_vector_type(2)));
typedef u32 u32x4 __attribute__((ext_vector_type(4)));
typedef float f32x2 __attribute__((ext_vector_type(2)));
typedef float f32x4 __attribute__((ext_vector_type(4)));
typedef short s16x4 __attribute__((ext_vector_type(4)));
typedef __bf16 bf16x8 __attribute__((ext_vector_type(8)));
#define LAS __attribute__((address_space(3)))
#define GAS __attribute__((address_space(1)))
#define RLX_AGENT __ATOMIC_RELAXED, __HIP_MEMORY_SCOPE_AGENT

#define KV_NT 1
#define WIN 6
#define TSLEEP 100
#define WQ_ON 1
#define QTRIG 16
#define MLTRIG 62
#define WO_ON 1
#define CODE_PF 1
#define W_NT 1
#define CODE_BYTES 18000
#define PARK 32

constexpr int NB = 8, DM = 512, HKV = 8, SK = 4096, DH = 128, FF = 1792;
constexpr int NWG = 256, NSW = 8, NHW = 4, NTHR = 64 * (NSW + NHW);
constexpr int NE = 24, E_LO = 132 - NE, NT_HALF = NE * 128, NTAB = 2 * NT_HALF;
static_assert(NTAB == NWG * 24, "24 table entries per workgroup");
constexpr float SCALE = 0.08838834764831845f, LOG2E = 1.4426950408889634f;
constexpr u32 SPIN_CAP = 1u << 15;

constexpr u32 WS_EPOCH = 0, WS_QG = 4096, WS_ML = WS_QG + 131072, WS_PA = WS_ML + 16384, WS_PB = WS_PA + 524288, WS_TB = WS_PB + 262144,
              WS_END = WS_TB + NTAB * 8, WS_DBG = 2u << 20;
constexpr u32 L_QF = 0, L_CTL = 1024, L_MX = 1088, L_LW = 1216, L_FS = 1344, L_RED2 = 2048, L_HROW = L_RED2 + 4096, L_RED = L_HROW + 2048,
              L_TAB = L_RED + 16384, L_WQ = L_TAB + NTAB * 4, L_END = L_WQ + 16384;

__device__ __forceinline__ float bflo(u32 w) { return __builtin_bit_cast(float, w << 16); }
__device__ __forceinline__ float bfhi(u32 w) { return __builtin_bit_cast(float, w & 0xffff0000u); }
__device__ __forceinline__ u32 cvtpk(float lo, float hi) {
  typedef __bf16 bf16x2 __attribute__((ext_vector_type(2)));
  bf16x2 r = {(__bf16)lo, (__bf16)hi};
  return __builtin_bit_cast(u32, r);
}
__device__ __forceinline__ float rbf(float f) { return bflo(cvtpk(f, f)); }
template <int CTRL> __device__ __forceinline__ float dppf(float x) {
  return __builtin_bit_cast(float, __builtin_amdgcn_mov_dpp(__builtin_bit_cast(int, x), CTRL, 0xf, 0xf, true));
}
template <int CTRL> __device__ __forceinline__ u32 dppu(u32 x) { return (u32)__builtin_amdgcn_mov_dpp((int)x, CTRL, 0xf, 0xf, true); }
constexpr int XOR1 = 0xB1, XOR2 = 0x4E, HMIR = 0x141, MIR = 0x140;
__device__ __forceinline__ float xrow_sum(float x) {
  auto s = __builtin_amdgcn_permlane16_swap(__float_as_uint(x), __float_as_uint(x), false, false);
  x = __uint_as_float(s[0]) + __uint_as_float(s[1]);
  auto t = __builtin_amdgcn_permlane32_swap(__float_as_uint(x), __float_as_uint(x), false, false);
  return __uint_as_float(t[0]) + __uint_as_float(t[1]);
}
__device__ __forceinline__ float xrow_max(float x) {
  auto s = __builtin_amdgcn_permlane16_swap(__float_as_uint(x), __float_as_uint(x), false, false);
  x = fmaxf(__uint_as_float(s[0]), __uint_as_float(s[1]));
  auto t = __builtin_amdgcn_permlane32_swap(__float_as_uint(x), __float_as_uint(x), false, false);
  return fmaxf(__uint_as_float(t[0]), __uint_as_float(t[1]));
}
__device__ __forceinline__ float wave_sum(float x) {
  x += dppf<XOR1>(x); x += dppf<XOR2>(x); x += dppf<HMIR>(x); x += dppf<MIR>(x);
  return xrow_sum(x);
}
__device__ __forceinline__ f32x4 mfma32(u32x4 a, u32x4 b, f32x4 c) {
  return __builtin_amdgcn_mfma_f32_16x16x32_bf16(__builtin_bit_cast(bf16x8, a), __builtin_bit_cast(bf16x8, b), c, 0, 0, 0);
}
__device__ __forceinline__ f32x4 mfma16(u32x2 a, u32x2 b, f32x4 c) {
  return __builtin_amdgcn_mfma_f32_16x16x16bf16_1k(__builtin_bit_cast(s16x4, a), __builtin_bit_cast(s16x4, b), c, 0, 0, 0);
}
template <int N> __device__ __forceinline__ void wait_vm() { asm volatile("s_waitcnt vmcnt(%0)" ::"n"(N) : "memory"); }
__device__ __forceinline__ void wait_lgkm() { asm volatile("s_waitcnt lgkmcnt(0)" ::: "memory"); }
__device__ __forceinline__ void bar() { asm volatile("s_waitcnt lgkmcnt(0)\n\ts_barrier" ::: "memory"); }
#define SB() __builtin_amdgcn_sched_barrier(0)
__device__ __forceinline__ u32x4 ldkv(const u32x4* p) {
  return __builtin_nontemporal_load(p);
}
__device__ __forceinline__ u32x4 ldw(const u32x4* p) {
  return __builtin_nontemporal_load(p);
}
__device__ __forceinline__ void st_gran(unsigned char* ws, u32 off, u32 tag, u32 val) {
  __hip_atomic_store((GAS u64*)(ws + off), ((u64)tag << 32) | val, RLX_AGENT);
}
__device__ __forceinline__ u64 ld_gran(unsigned char* ws, u32 off) { return __hip_atomic_load((GAS u64*)(ws + off), RLX_AGENT); }
__device__ __forceinline__ u32x4 ld_gran2(__amdgpu_buffer_rsrc_t rs, u32 off) { return __builtin_amdgcn_raw_buffer_load_b128(rs, off, 0, 16); }

#define STAMPR(i) do {} while (0)
#define STAMPW() do {} while (0)
#define STAMP(i) do {} while (0)

constexpr float GK0 = -2.302208198f, GK1 = GK0 * 0.044715f;
__device__ __forceinline__ float gelu_term(float t, float w1, float b1, float w2, float acc) {
  const float u = fmaf(t, w1, b1);
  const float z = u * fmaf(u * u, GK1, GK0);
  const float e = __builtin_amdgcn_exp2f(z);
  return fmaf(u * w2, __builtin_amdgcn_rcpf(1.0f + e), acc);
}

__device__ __forceinline__ void ln_row(float (&v)[8]) {
  float s = 0.f;
#pragma unroll
  for (int e = 0; e < 8; ++e) s += v[e];
  const float mean = wave_sum(s) * (1.0f / DM);
  float q = 0.f;
#pragma unroll
  for (int e = 0; e < 8; ++e) { const float d = v[e] - mean; q = fmaf(d, d, q); }
  const float var = rbf(wave_sum(q) * (1.0f / DM));
  const float mb = rbf(mean);
  const float den = rbf(__builtin_amdgcn_sqrtf(rbf(var + 1e-5f)));
  const float inv = __builtin_amdgcn_rcpf(den);
#pragma unroll
  for (int e = 0; e < 8; ++e) v[e] = rbf(rbf(v[e] - mb) * inv);
}

extern "C" __global__ void __launch_bounds__(NTHR) fused_kernel(const u16* x, const u16* kc, const u16* vc, const u16* wq, const u16* wo, const u16* w1,
                                                     const u16* b1, const u16* w2, const u16* b2, u16* out, unsigned char* ws) {
  __shared__ __attribute__((aligned(16))) unsigned char lds[L_END];
  const int tid = threadIdx.x, lane = tid & 63, wave = __builtin_amdgcn_readfirstlane(tid >> 6);
  const int wg = blockIdx.x, kvh = wg & 7, sp = (wg >> 3) & 3, b = wg >> 5;
  const bool final_wg = (kvh == b) && (sp == 0);
  const int li = lane & 15, lg = lane >> 4;
  volatile LAS u32* ctl = (volatile LAS u32*)(lds + L_CTL);
  const __amdgpu_buffer_rsrc_t rs = __builtin_amdgcn_make_buffer_rsrc(ws, 0, WS_END, 0x00020000);
  STAMP(0);

  if (wave < NSW) {
    const size_t tile = ((size_t)(b * HKV + kvh) * SK + sp * 1024) * 16;
    const u32x4* kb = (const u32x4*)kc + tile + wave * 256;
    const u32x4* vb = (const u32x4*)vc + tile + wave * 256;
    const u32x4* wb = (const u32x4*)wo + (size_t)(kvh * 512 + 64 * wave) * 64 + 16 * sp;
    const u32 klane = ((lane >> 2) & 3) * 16 + 4 * lg + (lane & 3);
    const u32 wlane = lg * 8 * 64 + li;
    u32x4 K[32], V[32], WO[16];
    auto issue = [&](auto nc) {
      constexpr int n = decltype(nc)::value;
      if constexpr (n < 32) {
        K[n] = ldkv(kb + (n >> 2) * 2048 + (n & 3) * 64 + klane);
      } else {
        constexpr int m = n - 32, grp = m / 3, r = m % 3;
        if constexpr (r < 2) { constexpr int v = grp * 2 + r; V[v] = ldkv(vb + (v >> 2) * 2048 + (v & 3) * 64 + lane); }
        else { WO[grp] = ldw(wb + (((grp >> 3) * 32 + (grp & 7)) * 64 + wlane) * WO_ON); }
      }
    };
    auto for_range = [&](auto lo, auto hi, auto&& f) {
      constexpr int L = decltype(lo)::value, H = decltype(hi)::value;
      [&]<int... I>(std::integer_sequence<int, I...>) { (f(std::integral_constant<int, L + I>{}), ...); }(std::make_integer_sequence<int, (H > L ? H - L : 0)>{});
    };
#define IC(n) std::integral_constant<int, (n)>{}
    for_range(IC(0), IC(WIN), [&](auto n) { issue(n); });
    SB();
    if (tid < 8) ctl[tid] = 0;
    bar();
    SB();
    STAMPR(0);
    for_range(IC(WIN), IC(PARK), [&](auto n) { wait_vm<WIN - 1>(); SB(); if constexpr (decltype(n)::value == WIN) STAMPR(1); if constexpr (decltype(n)::value == WIN + 6) STAMPR(2); if constexpr (decltype(n)::value == WIN + 12) STAMPR(3); issue(n); if constexpr (decltype(n)::value == QTRIG) ctl[4] = 1; SB(); });
    STAMPW();
    STAMP(1);
    for (u32 spins = 0; ctl[0] == 0 && spins < (SPIN_CAP << 4); ++spins) __builtin_amdgcn_s_sleep(1);
    asm volatile("" ::: "memory");
    const u32 tag = __builtin_amdgcn_readfirstlane(ctl[1]);
    const u32x4 qf = *(const LAS u32x4*)(lds + L_QF + lane * 16);
    STAMP(2);
    const bool c0 = lane & 1, c1 = lane & 2;
    float S[8];
    u32 pp[8];
    f32x4 acc0 = {0.f, 0.f, 0.f, 0.f}, acc1 = {0.f, 0.f, 0.f, 0.f};
    const u32 half = c0 ? 0xffff0000u : 0x0000ffffu;
    const u32 mask0 = c1 ? 0u : half, mask1 = c1 ? half : 0u;
    u32x4 WB[2][8];
    auto consume_kgroup = [&](auto uc) {
      constexpr int u = decltype(uc)::value;
      const f32x4 z = {0.f, 0.f, 0.f, 0.f};
      float v[4];
#pragma unroll
      for (int t = 0; t < 4; ++t) {
        const f32x4 d = mfma32(K[4 * u + t], qf, z);
        const float lo = c0 ? d[1] : d[0], hi = c0 ? d[3] : d[2];
        v[t] = c1 ? hi : lo;
      }
      const float w0 = (c0 ? v[1] : v[0]) + dppf<XOR1>(c0 ? v[0] : v[1]);
      const float w1_ = (c0 ? v[3] : v[2]) + dppf<XOR1>(c0 ? v[2] : v[3]);
      S[u] = rbf(rbf((c1 ? w1_ : w0) + dppf<XOR2>(c1 ? w0 : w1_)) * SCALE);
    };
    auto softmax = [&]() {
      float m = S[0];
#pragma unroll
      for (int u = 1; u < 8; ++u) m = fmaxf(m, S[u]);
      m = fmaxf(m, dppf<XOR1>(m)); m = fmaxf(m, dppf<XOR2>(m)); m = xrow_max(m);
      const float mc = m * LOG2E;
      float l = 0.f;
#pragma unroll
      for (int u = 0; u < 8; ++u) {
        const float p = __builtin_amdgcn_exp2f(fmaf(S[u], LOG2E, -mc));
        pp[u] = cvtpk(p, p);
        l += bflo(pp[u]);
      }
      l += dppf<XOR1>(l); l += dppf<XOR2>(l); l = xrow_sum(l);
      if ((lane & 3) == 0 && lg == 0) {
        *(LAS float*)(lds + L_MX + ((li >> 2) * 8 + wave) * 4) = m;
        *(LAS float*)(lds + L_LW + ((li >> 2) * 8 + wave) * 4) = l;
      }
      wait_lgkm();
      if (lane == 0) __hip_atomic_fetch_add((LAS u32*)(lds + L_CTL + 12), 1u, __ATOMIC_RELAXED, __HIP_MEMORY_SCOPE_WORKGROUP);
    };
    auto consume_v = [&](auto vc_) {
      constexpr int v = decltype(vc_)::value, u = v >> 2, t = v & 3;
      const u32 pb = dppu<t * 0x55>(pp[u]);
      const u32x2 a = {pb & mask0, pb & mask1};
      acc0 = mfma16(a, u32x2{V[v][0], V[v][1]}, acc0);
      acc1 = mfma16(a, u32x2{V[v][2], V[v][3]}, acc1);
    };
    auto perm_wo = [&](auto ksc) {
      constexpr int ks = decltype(ksc)::value;
#pragma unroll
      for (int c = 0; c < 8; ++c)
#pragma unroll
        for (int d = 0; d < 4; ++d)
          WB[ks][c][d] = __builtin_amdgcn_perm(WO[ks * 8 + 2 * d + 1][c >> 1], WO[ks * 8 + 2 * d][c >> 1], (c & 1) ? 0x07060302u : 0x05040100u);
    };
    auto consume = [&](auto ic) {
      constexpr int i = decltype(ic)::value;
      if constexpr (i < 32) {
        if constexpr ((i & 3) == 3) consume_kgroup(IC(i >> 2));
        if constexpr (i == 31) { STAMP(3); softmax(); STAMP(4); }
      } else {
        constexpr int m = i - 32, grp = m / 3, r = m % 3;
        if constexpr (r < 2) consume_v(IC(grp * 2 + r));
        else if constexpr ((grp & 7) == 7) perm_wo(IC(grp >> 3));
      }
    };
    for_range(IC(0), IC(PARK - WIN), [&](auto i) { consume(i); });
    SB();
    for_range(IC(PARK), IC(80), [&](auto n) { consume(IC(decltype(n)::value - WIN)); SB(); wait_vm<WIN - 1>(); SB(); issue(n); if constexpr (decltype(n)::value == MLTRIG) ctl[5] = 1; SB(); });
    for_range(IC(80 - WIN), IC(80), [&](auto i) { consume(i); SB(); });
    STAMP(5);
    *(LAS f32x4*)(lds + L_RED + wave * 2048 + lane * 32) = acc0;
    *(LAS f32x4*)(lds + L_RED + wave * 2048 + lane * 32 + 16) = acc1;
    bar();
    const u32 grp_off = (u32)(b * HKV + kvh);
    if (wave < 4) {
      const f32x4 fa = *(const LAS f32x4*)(lds + L_FS + wave * 32), fb = *(const LAS f32x4*)(lds + L_FS + wave * 32 + 16);
      f32x2 s = *(const LAS f32x2*)(lds + L_RED + tid * 8) * fa[0];
#pragma unroll
      for (int w = 1; w < 8; ++w) s += *(const LAS f32x2*)(lds + L_RED + w * 2048 + tid * 8) * (w < 4 ? fa[w & 3] : fb[w & 3]);
      st_gran(ws, WS_PA + (grp_off * 4 + sp) * 2048 + tid * 8, tag, cvtpk(s[0], s[1]));
    }
    STAMP(6);
    u32x4 A[2];
    {
      const u32 off = WS_PA + (grp_off * 4 + (li & 3)) * 2048 + (32 * wave + 4 * lg) * 8 + (li >> 3) * 128 + ((li >> 2) & 1) * 16;
      u32x4 g;
      bool ok = false;
      for (u32 spins = 0;; ++spins) {
        if (!ok) {
          g = ld_gran2(rs, off);
          ok = g[1] == tag && g[3] == tag;
        }
        if (__all(ok) || spins > SPIN_CAP) break;
        __builtin_amdgcn_s_sleep(2);
      }
      A[0] = u32x4{g[0], g[2], dppu<0x104>(g[0]), dppu<0x104>(g[2])};
      A[1] = u32x4{dppu<0x108>(g[0]), dppu<0x108>(g[2]), dppu<0x10C>(g[0]), dppu<0x10C>(g[2])};
    }
    STAMP(7);
    {
      float o[8];
#pragma unroll
      for (int c = 0; c < 8; ++c) {
        f32x4 d = {0.f, 0.f, 0.f, 0.f};
        d = mfma32(A[0], WB[0][c], d);
        d = mfma32(A[1], WB[1][c], d);
        o[c] = (d[0] + d[1]) + (d[2] + d[3]);
      }
      if (lg == 0) {
        *(LAS f32x4*)(lds + L_RED2 + wave * 512 + li * 32) = f32x4{o[0], o[1], o[2], o[3]};
        *(LAS f32x4*)(lds + L_RED2 + wave * 512 + li * 32 + 16) = f32x4{o[4], o[5], o[6], o[7]};
      }
    }
    bar();
    if (wave < 2) {
      float s = *(const LAS float*)(lds + L_RED2 + tid * 4);
#pragma unroll
      for (int w = 1; w < 8; ++w) s += *(const LAS float*)(lds + L_RED2 + w * 512 + tid * 4);
      st_gran(ws, WS_PB + grp_off * 4096 + (128 * sp + tid) * 8, tag, __float_as_uint(s));
    }
    STAMP(8);
    if (!final_wg) return;
    const float xv = bflo(x[b * DM + tid]);
    float a;
    for (u32 spins = 0;; ++spins) {
      u64 g[8];
#pragma unroll
      for (int k = 0; k < 8; ++k) g[k] = ld_gran(ws, WS_PB + (b * HKV + k) * 4096 + tid * 8);
      bool ok = true;
      a = 0.f;
#pragma unroll
      for (int k = 0; k < 8; ++k) { ok &= (u32)(g[k] >> 32) == tag; a += __uint_as_float((u32)g[k]); }
      if (__all(ok) || spins > SPIN_CAP) break;
      __builtin_amdgcn_s_sleep(1);
    }
    STAMP(9);
    const float hv = rbf(xv + rbf(a));
    *(LAS float*)(lds + L_HROW + tid * 4) = hv;
    bar();
    float tv;
    {
      const f32x4 h0 = *(const LAS f32x4*)(lds + L_HROW + lane * 32), h1 = *(const LAS f32x4*)(lds + L_HROW + lane * 32 + 16);
      const float piv = *(const LAS float*)(lds + L_HROW);
      float v[8];
#pragma unroll
      for (int e = 0; e < 4; ++e) { v[e] = h0[e]; v[4 + e] = h1[e]; }
      float s1 = 0.f, q1 = 0.f;
#pragma unroll
      for (int e = 0; e < 8; ++e) { const float d = v[e] - piv; s1 += d; q1 = fmaf(d, d, q1); }
      s1 = wave_sum(s1) * (1.0f / DM); q1 = wave_sum(q1) * (1.0f / DM);
      const float mb1 = rbf(piv + s1);
      const float inv1 = __builtin_amdgcn_rcpf(rbf(__builtin_amdgcn_sqrtf(rbf(rbf(fmaxf(fmaf(-s1, s1, q1), 0.f)) + 1e-5f))));
      float s2 = 0.f, q2 = 0.f;
#pragma unroll
      for (int e = 0; e < 8; e += 2) {
        const u32 d = cvtpk(v[e] - mb1, v[e + 1] - mb1);
        const u32 n = cvtpk(bflo(d) * inv1, bfhi(d) * inv1);
        const float n0 = bflo(n), n1 = bfhi(n);
        s2 += n0 + n1; q2 = fmaf(n0, n0, q2); q2 = fmaf(n1, n1, q2);
      }
      const float t1 = rbf(rbf(hv - mb1) * inv1);
      s2 = wave_sum(s2) * (1.0f / DM); q2 = wave_sum(q2) * (1.0f / DM);
      const float inv2 = __builtin_amdgcn_rcpf(rbf(__builtin_amdgcn_sqrtf(rbf(rbf(fmaxf(fmaf(-s2, s2, q2), 0.f)) + 1e-5f))));
      tv = rbf(rbf(t1 - rbf(s2)) * inv2);
    }
    const u32 bits = __float_as_uint(tv) >> 16;
    const u32 rel = (bits & 0x7fffu) - (E_LO << 7);
    const bool in = rel < (u32)NT_HALF;
    float y = *(const LAS float*)(lds + L_TAB + ((in ? rel : 0u) + (bits >> 15) * NT_HALF) * 4);
    u64 mm = __ballot(!in);
    while (mm) {
      const int src = __builtin_ctzll(mm);
      mm &= mm - 1;
      const float ts = __builtin_bit_cast(float, __builtin_amdgcn_readlane(__float_as_uint(tv), src));
      float acc = 0.f;
#pragma unroll 1
      for (int f = lane; f < FF; f += 64) acc = gelu_term(ts, bflo(w1[f]), bflo(b1[f]), bflo(w2[f]), acc);
      acc = wave_sum(acc);
      if (lane == src) y = rbf(rbf(acc) + bflo(b2[0]));
    }
    out[b * DM + tid] = (u16)cvtpk(hv + y, 0.f);
    if (b == 0 && tid == 0) __hip_atomic_store((GAS u32*)(ws + WS_EPOCH), tag, RLX_AGENT);
    STAMP(10);
    return;
  }

  __builtin_amdgcn_s_setprio(3);
  bar();
  SB();
  if (wave == NSW) {
    const int i32 = wg >> 3;
    u32x4 xa[8], wt[16];
    const u32x4* xp = (const u32x4*)x + (li & 7) * 64 + (li >> 3) * 4 + lg;
    const u32x4* wp = (const u32x4*)(wq + kvh * 512 + 16 * i32) + (size_t)(lane >> 1) * 512 + (lane & 1);
#pragma unroll
    for (int k = 0; k < 8; ++k) xa[k] = xp[k * 8];
#pragma unroll
    for (int k = 0; k < 16; ++k) wt[k] = ldw(wp + (size_t)k * 32 * 512 * WQ_ON);
    const u32 epoch = *(const __attribute__((address_space(4))) u32*)(ws + WS_EPOCH);
    const u32 tag = epoch + 1u;
    {
      float sm = 0.f;
#pragma unroll
      for (int k = 0; k < 8; ++k)
#pragma unroll
        for (int e = 0; e < 4; ++e) sm += bflo(xa[k][e]) + bfhi(xa[k][e]);
      sm += dppf<0x128>(sm);
      const float mean = xrow_sum(sm) * (1.0f / DM);
      float sq = 0.f;
#pragma unroll
      for (int k = 0; k < 8; ++k)
#pragma unroll
        for (int e = 0; e < 4; ++e) { const float d0 = bflo(xa[k][e]) - mean, d1 = bfhi(xa[k][e]) - mean; sq = fmaf(d0, d0, sq); sq = fmaf(d1, d1, sq); }
      sq += dppf<0x128>(sq);
      const float var = rbf(xrow_sum(sq) * (1.0f / DM)), mb = rbf(mean);
      const float inv = __builtin_amdgcn_rcpf(rbf(__builtin_amdgcn_sqrtf(rbf(var + 1e-5f))));
#pragma unroll
      for (int k = 0; k < 8; ++k)
#pragma unroll
        for (int e = 0; e < 4; ++e) {
          const u32 d = cvtpk(bflo(xa[k][e]) - mb, bfhi(xa[k][e]) - mb);
          xa[k][e] = cvtpk(bflo(d) * inv, bfhi(d) * inv);
        }
    }
    STAMP(4);
#pragma unroll
    for (int k = 0; k < 16; ++k) *(LAS u32x4*)(lds + L_WQ + k * 1024 + lane * 16) = wt[k];
    wait_lgkm();
    STAMP(5);
    f32x4 dq0 = {0.f, 0.f, 0.f, 0.f}, dq1 = {0.f, 0.f, 0.f, 0.f};
    const u32 troff = L_WQ + (8 * lg + (li >> 2)) * 32 + (li & 3) * 8;
#pragma unroll
    for (int k = 0; k < 16; ++k) {
      const s16x4 b0 = __builtin_amdgcn_ds_read_tr16_b64_v4i16((LAS s16x4*)(lds + troff + k * 1024));
      const s16x4 b1_ = __builtin_amdgcn_ds_read_tr16_b64_v4i16((LAS s16x4*)(lds + troff + k * 1024 + 128));
      const u32x2 p0 = __builtin_bit_cast(u32x2, b0), p1 = __builtin_bit_cast(u32x2, b1_);
      if (k & 1) dq1 = mfma32(xa[k >> 1], u32x4{p0[0], p0[1], p1[0], p1[1]}, dq1);
      else dq0 = mfma32(xa[k >> 1], u32x4{p0[0], p0[1], p1[0], p1[1]}, dq0);
    }
    {
      const bool odd = lane & 1, up = lg & 2;
      const float k0 = up ? dq1[2] : dq0[0], k1 = up ? dq1[3] : dq0[1];
      const float g0 = up ? dq1[0] : dq0[2], g1 = up ? dq1[1] : dq0[3];
      const float e0 = k0 + __shfl_xor(g0, 32), e1 = k1 + __shfl_xor(g1, 32);
      const float mine = odd ? e1 : e0, give = odd ? e0 : e1;
      const float got = dppf<XOR1>(give);
      const u32 val = odd ? cvtpk(got, mine) : cvtpk(mine, got);
      const int row = 4 * (lg & 1) + (lg & 2) + (lane & 1);
      st_gran(ws, WS_QG + (row * HKV + kvh) * 2048 + (8 * i32 + (li >> 1)) * 8, tag, val);
    }
    STAMP(1);
    {
      const u32 off = WS_QG + (b * HKV + kvh) * 2048 + ((li >> 2) * 64 + 16 * lg + 4 * (li & 3)) * 8;
      u32x4 q4;
      for (u32 spins = 0; ctl[4] == 0 && spins < (SPIN_CAP << 4); ++spins) __builtin_amdgcn_s_sleep(2);
      asm volatile("" ::: "memory");
      for (u32 spins = 0;; ++spins) {
        const u32x4 g0 = ld_gran2(rs, off), g1 = ld_gran2(rs, off + 16);
        const bool ok = g0[1] == tag && g0[3] == tag && g1[1] == tag && g1[3] == tag;
        q4 = u32x4{g0[0], g0[2], g1[0], g1[2]};
        if (__all(ok) || spins > SPIN_CAP) break;
        __builtin_amdgcn_s_sleep(8);
      }
      *(LAS u32x4*)(lds + L_QF + lane * 16) = q4;
      if (lane == 0) ctl[1] = tag;
      wait_lgkm();
      if (lane == 0) ctl[0] = 1;
    }
    STAMP(2);
    for (u32 spins = 0; ctl[3] < 8 && spins < (SPIN_CAP << 4); ++spins) __builtin_amdgcn_s_sleep(4);
    asm volatile("" ::: "memory");
    {
      const float mw = *(const LAS float*)(lds + L_MX + (lane & 31) * 4), lw = *(const LAS float*)(lds + L_LW + (lane & 31) * 4);
      float M = fmaxf(mw, dppf<XOR1>(mw)); M = fmaxf(M, dppf<XOR2>(M)); M = fmaxf(M, dppf<HMIR>(M));
      float l = lw * __builtin_amdgcn_exp2f((mw - M) * LOG2E);
      l += dppf<XOR1>(l); l += dppf<XOR2>(l); l += dppf<HMIR>(l);
      const u32 goff = WS_ML + (u32)(b * HKV + kvh) * 256;
      if (lane < 32 && (lane & 7) < 2) st_gran(ws, goff + sp * 64 + ((lane >> 3) * 2 + (lane & 1)) * 8, tag, __float_as_uint((lane & 1) ? l : M));
      u32x4 g;
      for (u32 spins = 0; ctl[5] == 0 && spins < (SPIN_CAP << 4); ++spins) __builtin_amdgcn_s_sleep(4);
      asm volatile("" ::: "memory");
      for (u32 spins = 0;; ++spins) {
        g = ld_gran2(rs, goff + (lane & 3) * 64 + ((lane >> 3) & 3) * 16);
        if (__all(g[1] == tag && g[3] == tag) || spins > SPIN_CAP) break;
        __builtin_amdgcn_s_sleep(8);
      }
      const float Mp = __uint_as_float(g[0]);
      float Ms = fmaxf(Mp, dppf<XOR1>(Mp)); Ms = fmaxf(Ms, dppf<XOR2>(Ms));
      float L = __uint_as_float(g[2]) * __builtin_amdgcn_exp2f((Mp - Ms) * LOG2E);
      L += dppf<XOR1>(L); L += dppf<XOR2>(L);
      if (lane < 32) *(LAS float*)(lds + L_FS + lane * 4) = __builtin_amdgcn_exp2f((mw - Ms) * LOG2E) / L;
    }
    STAMP(3);
    bar();
    return;
  }
  {
    const u32 epoch = *(const __attribute__((address_space(4))) u32*)(ws + WS_EPOCH);
    u32 pf = 0;
    if (wave == NSW + 1) {
      u64 pc; u32 back;
      constexpr u32 len = CODE_BYTES;
      asm volatile("s_getpc_b64 %0\n.Lpf_here:\n\ts_mov_b32 %1, .Lpf_here-fused_kernel" : "=s"(pc), "=s"(back));
      const u32 o = ((wg >> 3) * 8 + (lane & 7) + (lane >> 3) * 256) * 128;
      if (o + 4 <= len) pf = *(const GAS u32*)(pc - back + o);
    }
    __builtin_amdgcn_s_sleep(TSLEEP);
    asm volatile("" ::: "memory");
    u32x2 r1[7], rb[7], r2[7];
#pragma unroll
    for (int c = 0; c < 7; ++c) {
      r1[c] = ((const u32x2*)w1)[c * 64 + lane];
      rb[c] = ((const u32x2*)b1)[c * 64 + lane];
      r2[c] = ((const u32x2*)w2)[c * 64 + lane];
    }
    const float b2v = bflo(b2[0]);
    const u32 tag = epoch + 1u;
    const int idx0 = wg * 24 + (wave - NSW - 1) * 8;
    f32x2 f1[14], fb[14], f2[14];
#pragma unroll
    for (int c = 0; c < 14; ++c) {
      f1[c] = f32x2{bflo(r1[c >> 1][c & 1]), bfhi(r1[c >> 1][c & 1])};
      fb[c] = f32x2{bflo(rb[c >> 1][c & 1]), bfhi(rb[c >> 1][c & 1])};
      f2[c] = f32x2{bflo(r2[c >> 1][c & 1]), bfhi(r2[c >> 1][c & 1])};
    }
    float res = 0.f;
#pragma unroll 1
    for (int e = 0; e < 8; ++e) {
      const int i0 = idx0 + e;
      const float t0 = __uint_as_float((u32)((i0 >= NT_HALF ? i0 - NT_HALF + 0x8000 : i0) + (E_LO << 7)) << 16);
      const f32x2 tt = {t0, t0};
      f32x2 acc = {0.f, 0.f};
#pragma unroll
      for (int c0_ = 0; c0_ < 14; c0_ += 7) {
        f32x2 u[7], z[7];
#pragma unroll
        for (int c = 0; c < 7; ++c) u[c] = __builtin_elementwise_fma(tt, f1[c0_ + c], fb[c0_ + c]);
        SB();
#pragma unroll
        for (int c = 0; c < 7; ++c) z[c] = u[c] * u[c];
        SB();
#pragma unroll
        for (int c = 0; c < 7; ++c) z[c] = __builtin_elementwise_fma(z[c], f32x2{GK1, GK1}, f32x2{GK0, GK0});
        SB();
#pragma unroll
        for (int c = 0; c < 7; ++c) z[c] = z[c] * u[c];
        SB();
#pragma unroll
        for (int c = 0; c < 7; ++c) z[c] = f32x2{__builtin_amdgcn_exp2f(z[c][0]), __builtin_amdgcn_exp2f(z[c][1])};
        SB();
#pragma unroll
        for (int c = 0; c < 7; ++c) { z[c] = z[c] + f32x2{1.f, 1.f}; u[c] = u[c] * f2[c0_ + c]; }
        SB();
#pragma unroll
        for (int c = 0; c < 7; ++c) z[c] = f32x2{__builtin_amdgcn_rcpf(z[c][0]), __builtin_amdgcn_rcpf(z[c][1])};
        SB();
#pragma unroll
        for (int c = 0; c < 7; ++c) acc = __builtin_elementwise_fma(u[c], z[c], acc);
        SB();
      }
      const float a0 = wave_sum(acc[0] + acc[1]);
      if (lane == e) res = a0;
    }
    if (lane < 8) st_gran(ws, WS_TB + (idx0 + lane) * 8, tag, __float_as_uint(rbf(rbf(res) + b2v)));
    asm volatile("" ::"v"(pf));
    STAMP(1);
    if (final_wg) {
      for (u32 spins = 0; ctl[3] == 0 && spins < (SPIN_CAP << 4); ++spins) __builtin_amdgcn_s_sleep(8);
      asm volatile("" ::: "memory");
      const u32 base = (u32)(wave - NSW - 1) * 1024 + lane;
#pragma unroll 1
      for (int half = 0; half < 2; ++half) {
        for (u32 spins = 0;; ++spins) {
          u32x4 g[8];
          bool ok = true;
#pragma unroll
          for (int k = 0; k < 8; ++k) g[k] = ld_gran2(rs, WS_TB + (base + (half * 8 + k) * 64) * 16);
#pragma unroll
          for (int k = 0; k < 8; ++k) {
            ok &= g[k][1] == tag && g[k][3] == tag;
            *(LAS u32x2*)(lds + L_TAB + (base + (half * 8 + k) * 64) * 8) = u32x2{g[k][0], g[k][2]};
          }
          if (__all(ok) || spins > SPIN_CAP) break;
          __builtin_amdgcn_s_sleep(8);
        }
      }
      wait_lgkm();
    }
    STAMP(2);
  }
}

extern "C" void kernel_launch(void* const* d_in, const int* in_sizes, int n_in, void* d_out, int out_size, void* d_ws, size_t ws_size,
                              hipStream_t stream) {
  fused_kernel<<<dim3(NWG), dim3(NTHR), 0, stream>>>((const u16*)d_in[0], (const u16*)d_in[1], (const u16*)d_in[2], (const u16*)d_in[3],
                                                     (const u16*)d_in[4], (const u16*)d_in[6], (const u16*)d_in[7], (const u16*)d_in[8],
                                                     (const u16*)d_in[9], (u16*)d_out, (unsigned char*)d_ws);
}
```

```cpp
#include <hip/hip_runtime.h>
#include <stdint.h>
#include <utility>
#include <type_traits>

typedef unsigned short u16;
typedef unsigned u32;
typedef unsigned long long u64;
typedef u32 u32x2 __attribute__((ext_vector_type(2)));
typedef u32 u32x4 __attribute__((ext_vector_type(4)));
typedef float f32x2 __attribute__((ext_vector_type(2)));
typedef float f32x4 __attribute__((ext_vector_type(4)));
typedef short s16x4 __attribute__((ext_vector_type(4)));
typedef __bf16 bf16x8 __attribute__((ext_vector_type(8)));
#define LAS __attribute__((address_space(3)))
#define GAS __attribute__((address_space(1)))
#define CAS __attribute__((address_space(4)))
#define RLX_AGENT __ATOMIC_RELAXED, __HIP_MEMORY_SCOPE_AGENT

#define K_NT 1
#define V_NT 1
#define W_NT 0
#define WIN 6
#define QTRIG 14
#define MLTRIG 62
#define TSLEEP 100
#define CODE_PF 1
#define CODE_BYTES 17500
#define CRATE 3
#define ASLEEP 2

constexpr int DM = 512, HKV = 8, SK = 4096, FF = 1792;
constexpr int NWG = 256, NSW = 8, NTHR = 768, PARK = 32, NLD = 80;
constexpr int NE = 24, E_LO = 132 - NE, NT_HALF = NE * 128, NTAB = 2 * NT_HALF;
static_assert(NTAB == NWG * 24, "24 table entries per workgroup");
constexpr float SCALE = 0.08838834764831845f, LOG2E = 1.4426950408889634f;
constexpr float GK0 = -2.302208198f, GK1 = GK0 * 0.044715f;
constexpr u32 SPIN_CAP = 1u << 15, LSPIN_CAP = 1u << 19;

constexpr u32 WS_EPOCH = 0, WS_QG = 4096, WS_ML = WS_QG + 1048576, WS_PA = WS_ML + 16384, WS_PB = WS_PA + 524288, WS_TB = WS_PB + 262144,
              WS_END = WS_TB + NTAB * 8, WS_DBG = 2u << 20;
constexpr u32 L_QF = 0, L_CTL = 1024, L_MX = 1088, L_LW = 1216, L_FS = 1344, L_RED2 = 2048, L_HROW = L_RED2 + 4096, L_RED = L_HROW + 2048,
              L_TAB = L_RED + 16384, L_WQ = L_TAB + NTAB * 4, L_END = L_WQ + 16384;
enum { C_Q = 0, C_ML = 1, C_QTRIG = 2, C_MLTRIG = 3, C_TAG = 4 };

__device__ __forceinline__ float bflo(u32 w) { return __builtin_bit_cast(float, w << 16); }
__device__ __forceinline__ float bfhi(u32 w) { return __builtin_bit_cast(float, w & 0xffff0000u); }
__device__ __forceinline__ u32 cvtpk(float lo, float hi) {
  typedef __bf16 bf16x2 __attribute__((ext_vector_type(2)));
  bf16x2 r = {(__bf16)lo, (__bf16)hi};
  return __builtin_bit_cast(u32, r);
}
__device__ __forceinline__ float rbf(float f) { return bflo(cvtpk(f, f)); }
template <int CTRL> __device__ __forceinline__ float dppf(float x) {
  return __builtin_bit_cast(float, __builtin_amdgcn_mov_dpp(__builtin_bit_cast(int, x), CTRL, 0xf, 0xf, true));
}
template <int CTRL> __device__ __forceinline__ u32 dppu(u32 x) { return (u32)__builtin_amdgcn_mov_dpp((int)x, CTRL, 0xf, 0xf, true); }
constexpr int XOR1 = 0xB1, XOR2 = 0x4E, HMIR = 0x141, MIR = 0x140, ROR8 = 0x128;
__device__ __forceinline__ float xrow_sum(float x) {
  auto s = __builtin_amdgcn_permlane16_swap(__float_as_uint(x), __float_as_uint(x), false, false);
  x = __uint_as_float(s[0]) + __uint_as_float(s[1]);
  auto t = __builtin_amdgcn_permlane32_swap(__float_as_uint(x), __float_as_uint(x), false, false);
  return __uint_as_float(t[0]) + __uint_as_float(t[1]);
}
__device__ __forceinline__ float xrow_max(float x) {
  auto s = __builtin_amdgcn_permlane16_swap(__float_as_uint(x), __float_as_uint(x), false, false);
  x = fmaxf(__uint_as_float(s[0]), __uint_as_float(s[1]));
  auto t = __builtin_amdgcn_permlane32_swap(__float_as_uint(x), __float_as_uint(x), false, false);
  return fmaxf(__uint_as_float(t[0]), __uint_as_float(t[1]));
}
__device__ __forceinline__ float wave_sum(float x) {
  x += dppf<XOR1>(x); x += dppf<XOR2>(x); x += dppf<HMIR>(x); x += dppf<MIR>(x);
  return xrow_sum(x);
}
__device__ __forceinline__ f32x4 mfma32(u32x4 a, u32x4 b, f32x4 c) {
  return __builtin_amdgcn_mfma_f32_16x16x32_bf16(__builtin_bit_cast(bf16x8, a), __builtin_bit_cast(bf16x8, b), c, 0, 0, 0);
}
__device__ __forceinline__ f32x4 mfma16(u32x2 a, u32x2 b, f32x4 c) {
  return __builtin_amdgcn_mfma_f32_16x16x16bf16_1k(__builtin_bit_cast(s16x4, a), __builtin_bit_cast(s16x4, b), c, 0, 0, 0);
}
template <int N> __device__ __forceinline__ void wait_vm() { asm volatile("s_waitcnt vmcnt(%0)" ::"n"(N) : "memory"); }
__device__ __forceinline__ void wait_lgkm() { asm volatile("s_waitcnt lgkmcnt(0)" ::: "memory"); }
__device__ __forceinline__ void bar() { asm volatile("s_waitcnt lgkmcnt(0)\n\ts_barrier" ::: "memory"); }
#define SB() __builtin_amdgcn_sched_barrier(0)
#define IC(n) std::integral_constant<int, (n)>{}
template <int L, typename F, int... I> __device__ __forceinline__ void for_impl(F&& f, std::integer_sequence<int, I...>) { (f(std::integral_constant<int, L + I>{}), ...); }
template <int L, int H, typename F> __device__ __forceinline__ void for_range(F&& f) { for_impl<L>(f, std::make_integer_sequence<int, (H > L ? H - L : 0)>{}); }
template <int NT> __device__ __forceinline__ u32x4 ldp(const u32x4* p) { if constexpr (NT) return __builtin_nontemporal_load(p); else return *p; }
__device__ __forceinline__ void st_gran(unsigned char* ws, u32 off, u32 tag, u32 val) {
  __hip_atomic_store((GAS u64*)(ws + off), ((u64)tag << 32) | val, RLX_AGENT);
}
__device__ __forceinline__ u64 ld_gran(unsigned char* ws, u32 off) { return __hip_atomic_load((GAS u64*)(ws + off), RLX_AGENT); }
__device__ __forceinline__ u32x4 ld_gran2(__amdgpu_buffer_rsrc_t rs, u32 off) { return __builtin_amdgcn_raw_buffer_load_b128(rs, off, 0, 16); }
template <int SL> __device__ __forceinline__ void lds_wait_ge(volatile LAS u32* w, u32 n) {
  for (u32 spins = 0; *w < n && spins < LSPIN_CAP; ++spins) __builtin_amdgcn_s_sleep(SL);
  asm volatile("" ::: "memory");
}

#define STAMPR(i) do {} while (0)
#define STAMPW() do {} while (0)
#define STAMP(i) do {} while (0)

__device__ __forceinline__ float gelu_term(float t, float w1, float b1, float w2, float acc) {
  const float u = fmaf(t, w1, b1);
  const float z = u * fmaf(u * u, GK1, GK0);
  const float e = __builtin_amdgcn_exp2f(z);
  return fmaf(u * w2, __builtin_amdgcn_rcpf(1.0f + e), acc);
}
constexpr int cons_target(int n) { return (n - WIN + 1) < (4 + CRATE * (n - PARK)) ? (n - WIN + 1) : (4 + CRATE * (n - PARK)); }

extern "C" __global__ void __launch_bounds__(NTHR) fused_kernel(const u16* x, const u16* kc, const u16* vc, const u16* wq, const u16* wo,
                                                                const u16* w1, const u16* b1, const u16* w2, const u16* b2, u16* out,
                                                                unsigned char* ws) {
  __shared__ __attribute__((aligned(16))) unsigned char lds[L_END];
  const int lane = threadIdx.x & 63, hwave = __builtin_amdgcn_readfirstlane(threadIdx.x >> 6);
  const int wg = blockIdx.x, kvh = wg & 7, sp = (wg >> 3) & 3, b = wg >> 5;
  const bool final_wg = (kvh == b) && (sp == 0);
  const int li = lane & 15, lg = lane >> 4;
  volatile LAS u32* ctl = (volatile LAS u32*)(lds + L_CTL);
  const __amdgpu_buffer_rsrc_t rs = __builtin_amdgcn_make_buffer_rsrc(ws, 0, WS_END, 0x00020000);
  const u32 grp_off = (u32)(b * HKV + kvh);

  if (hwave == 0) {
    __builtin_amdgcn_s_setprio(3);
    if (lane < 4) ctl[lane] = 0;
    bar();
    SB();
    const int cb = (wg >> 3) & 7, kq = wg >> 6;
    u32x4 xa[8], wt[16];
    const u32x4* xp = (const u32x4*)x + (li & 7) * 64 + (li >> 3) * 4 + lg;
    const u32x4* wp = (const u32x4*)(wq + (size_t)(128 * kq) * 4096 + kvh * 512 + 64 * cb) + (lane >> 3) * 512 + (lane & 7);
#pragma unroll
    for (int k = 0; k < 8; ++k) xa[k] = xp[((k + 2 * kq) & 7) * 8];
#pragma unroll
    for (int k = 0; k < 16; ++k) wt[k] = ldp<W_NT>(wp + k * 8 * 512);
    const u32 tag = *(const CAS u32*)(ws + WS_EPOCH) + 1u;
    SB();
    STAMPR(0);
    {
      float sm = 0.f;
#pragma unroll
      for (int k = 0; k < 8; ++k)
#pragma unroll
        for (int e = 0; e < 4; ++e) sm += bflo(xa[k][e]) + bfhi(xa[k][e]);
      sm += dppf<ROR8>(sm);
      const float mean = xrow_sum(sm) * (1.0f / DM);
      float sq = 0.f;
#pragma unroll
      for (int k = 0; k < 8; ++k)
#pragma unroll
        for (int e = 0; e < 4; ++e) { const float d0 = bflo(xa[k][e]) - mean, d1 = bfhi(xa[k][e]) - mean; sq = fmaf(d0, d0, sq); sq = fmaf(d1, d1, sq); }
      sq += dppf<ROR8>(sq);
      const float var = rbf(xrow_sum(sq) * (1.0f / DM)), mb = rbf(mean);
      const float inv = __builtin_amdgcn_rcpf(rbf(__builtin_amdgcn_sqrtf(rbf(var + 1e-5f))));
#pragma unroll
      for (int k = 0; k < 2; ++k)
#pragma unroll
        for (int e = 0; e < 4; ++e) {
          const u32 d = cvtpk(bflo(xa[k][e]) - mb, bfhi(xa[k][e]) - mb);
          xa[k][e] = cvtpk(bflo(d) * inv, bfhi(d) * inv);
        }
    }
    SB();
    STAMPR(1);
#pragma unroll
    for (int k = 0; k < 16; ++k) *(LAS u32x4*)(lds + L_WQ + k * 1024 + lane * 16) = wt[k];
    wait_lgkm();
    STAMPR(2);
    const u32 troff = L_WQ + (8 * lg + (li >> 2)) * 128 + (li & 3) * 8;
    const bool odd = lane & 1, up = lg & 2;
    const u32 qoff = WS_QG + ((4 * (lg & 1) + (lg & 2) + (lane & 1)) * HKV + kvh) * 16384 + kq * 4096 + (64 * cb + (li & 14)) * 8;
#pragma unroll
    for (int ct = 0; ct < 4; ++ct) {
      f32x4 d0 = {0.f, 0.f, 0.f, 0.f}, d1 = {0.f, 0.f, 0.f, 0.f};
#pragma unroll
      for (int k = 0; k < 4; ++k) {
        const s16x4 b0 = __builtin_amdgcn_ds_read_tr16_b64_v4i16((LAS s16x4*)(lds + troff + k * 4096 + ct * 32));
        const s16x4 b1_ = __builtin_amdgcn_ds_read_tr16_b64_v4i16((LAS s16x4*)(lds + troff + k * 4096 + ct * 32 + 512));
        const u32x2 p0 = __builtin_bit_cast(u32x2, b0), p1 = __builtin_bit_cast(u32x2, b1_);
        if (k & 1) d1 = mfma32(xa[k >> 1], u32x4{p0[0], p0[1], p1[0], p1[1]}, d1);
        else d0 = mfma32(xa[k >> 1], u32x4{p0[0], p0[1], p1[0], p1[1]}, d0);
      }
      const auto s0 = __builtin_amdgcn_permlane32_swap(__float_as_uint(d1[0]), __float_as_uint(d0[2]), false, false);
      const auto s1 = __builtin_amdgcn_permlane32_swap(__float_as_uint(d1[1]), __float_as_uint(d0[3]), false, false);
      const float e0 = up ? d1[2] + __uint_as_float(s0[0]) : d0[0] + __uint_as_float(s0[1]);
      const float e1 = up ? d1[3] + __uint_as_float(s1[0]) : d0[1] + __uint_as_float(s1[1]);
      const float mine = odd ? e1 : e0, got = dppf<XOR1>(odd ? e0 : e1);
      const u32x4 g = {__float_as_uint(odd ? got : mine), tag, __float_as_uint(odd ? mine : got), tag};
      __builtin_amdgcn_raw_buffer_store_b128(g, rs, qoff + ct * 128, 0, 16);
    }
    STAMPR(3);
    STAMPW();
    lds_wait_ge<2>(ctl + C_QTRIG, 1);
    STAMP(1);
    {
      const u32 off = WS_QG + grp_off * 16384 + lane * 16;
      for (u32 spins = 0;; ++spins) {
        u32x4 g[16];
#pragma unroll
        for (int k = 0; k < 16; ++k) g[k] = ld_gran2(rs, off + k * 1024);
        bool ok = true;
#pragma unroll
        for (int k = 0; k < 16; ++k) ok &= g[k][1] == tag && g[k][3] == tag;
#pragma unroll
        for (int h = 0; h < 4; ++h) {
          const float q0 = (__uint_as_float(g[h][0]) + __uint_as_float(g[4 + h][0])) + (__uint_as_float(g[8 + h][0]) + __uint_as_float(g[12 + h][0]));
          const float q1 = (__uint_as_float(g[h][2]) + __uint_as_float(g[4 + h][2])) + (__uint_as_float(g[8 + h][2]) + __uint_as_float(g[12 + h][2]));
          *(LAS u32*)(lds + L_QF + (h * 64 + lane) * 4) = cvtpk(q0, q1);
        }
        if (__all(ok) || spins > SPIN_CAP) break;
        __builtin_amdgcn_s_sleep(8);
      }
      if (lane == 0) ctl[C_TAG] = tag;
      wait_lgkm();
      if (lane == 0) ctl[C_Q] = 1;
    }
    STAMP(2);
    lds_wait_ge<4>(ctl + C_ML, NSW);
    {
      const float mw = *(const LAS float*)(lds + L_MX + (lane & 31) * 4), lw = *(const LAS float*)(lds + L_LW + (lane & 31) * 4);
      float M = fmaxf(mw, dppf<XOR1>(mw)); M = fmaxf(M, dppf<XOR2>(M)); M = fmaxf(M, dppf<HMIR>(M));
      float l = lw * __builtin_amdgcn_exp2f((mw - M) * LOG2E);
      l += dppf<XOR1>(l); l += dppf<XOR2>(l); l += dppf<HMIR>(l);
      const u32 goff = WS_ML + grp_off * 256;
      if (lane < 32 && (lane & 7) < 2) st_gran(ws, goff + sp * 64 + ((lane >> 3) * 2 + (lane & 1)) * 8, tag, __float_as_uint((lane & 1) ? l : M));
      STAMP(3);
      lds_wait_ge<4>(ctl + C_MLTRIG, 1);
      u32x4 g;
      for (u32 spins = 0;; ++spins) {
        g = ld_gran2(rs, goff + (lane & 3) * 64 + ((lane >> 3) & 3) * 16);
        if (__all(g[1] == tag && g[3] == tag) || spins > SPIN_CAP) break;
        __builtin_amdgcn_s_sleep(8);
      }
      const float Mp = __uint_as_float(g[0]);
      float Ms = fmaxf(Mp, dppf<XOR1>(Mp)); Ms = fmaxf(Ms, dppf<XOR2>(Ms));
      float L = __uint_as_float(g[2]) * __builtin_amdgcn_exp2f((Mp - Ms) * LOG2E);
      L += dppf<XOR1>(L); L += dppf<XOR2>(L);
      if (lane < 32) *(LAS float*)(lds + L_FS + lane * 4) = __builtin_amdgcn_exp2f((mw - Ms) * LOG2E) / L;
    }
    STAMP(4);
    bar();
    return;
  }

  if (hwave <= NSW) {
    const int wave = hwave - 1, tid = threadIdx.x - 64;
    bar();
    SB();
    const size_t tile = ((size_t)grp_off * SK + sp * 1024) * 16;
    const u32x4* kb = (const u32x4*)kc + tile + wave * 256;
    const u32x4* vb = (const u32x4*)vc + tile + wave * 256;
    const u32x4* wb = (const u32x4*)wo + (size_t)(kvh * 512 + 64 * wave) * 64 + 16 * sp;
    const u32 klane = ((lane >> 2) & 3) * 16 + 4 * lg + (lane & 3);
    const u32 wlane = lg * 8 * 64 + li;
    u32x4 K[32], V[32], WO[16];
    auto issue = [&](auto nc) {
      constexpr int n = decltype(nc)::value;
      if constexpr (n < 32) {
        K[n] = ldp<K_NT>(kb + (n >> 2) * 2048 + (n & 3) * 64 + klane);
      } else {
        constexpr int m = n - 32, grp = m / 3, r = m % 3;
        if constexpr (r < 2) { constexpr int v = grp * 2 + r; V[v] = ldp<V_NT>(vb + (v >> 2) * 2048 + (v & 3) * 64 + lane); }
        else { WO[grp] = ldp<W_NT>(wb + ((grp >> 3) * 32 + (grp & 7)) * 64 + wlane); }
      }
      if constexpr (n == QTRIG) ctl[C_QTRIG] = 1;
      if constexpr (n == MLTRIG) ctl[C_MLTRIG] = 1;
    };
    for_range<0, WIN>([&](auto n) { issue(n); });
    SB();
    STAMPR(0);
    for_range<WIN, PARK>([&](auto n) {
      wait_vm<WIN - 1>(); SB();
      if constexpr (decltype(n)::value == WIN) STAMPR(1);
      if constexpr (decltype(n)::value == WIN + 6) STAMPR(2);
      if constexpr (decltype(n)::value == WIN + 12) STAMPR(3);
      issue(n); SB();
    });
    STAMPW();
    STAMP(1);
    lds_wait_ge<1>(ctl + C_Q, 1);
    const u32 tag = __builtin_amdgcn_readfirstlane(ctl[C_TAG]);
    const u32x4 qf = *(const LAS u32x4*)(lds + L_QF + (li >> 2) * 256 + lg * 64 + (li & 3) * 16);
    STAMP(2);
    const bool c0 = lane & 1, c1 = lane & 2;
    float S[8];
    u32 pp[8];
    f32x4 acc0 = {0.f, 0.f, 0.f, 0.f}, acc1 = {0.f, 0.f, 0.f, 0.f};
    const u32 half = c0 ? 0xffff0000u : 0x0000ffffu;
    const u32 mask0 = c1 ? 0u : half, mask1 = c1 ? half : 0u;
    u32x4 WB[2][8];
    auto consume_kgroup = [&](auto uc) {
      constexpr int u = decltype(uc)::value;
      const f32x4 z = {0.f, 0.f, 0.f, 0.f};
      float v[4];
#pragma unroll
      for (int t = 0; t < 4; ++t) {
        const f32x4 d = mfma32(K[4 * u + t], qf, z);
        const float lo = c0 ? d[1] : d[0], hi = c0 ? d[3] : d[2];
        v[t] = c1 ? hi : lo;
      }
      const float w0 = (c0 ? v[1] : v[0]) + dppf<XOR1>(c0 ? v[0] : v[1]);
      const float w1_ = (c0 ? v[3] : v[2]) + dppf<XOR1>(c0 ? v[2] : v[3]);
      S[u] = rbf(rbf((c1 ? w1_ : w0) + dppf<XOR2>(c1 ? w0 : w1_)) * SCALE);
    };
    auto softmax = [&]() {
      float m = S[0];
#pragma unroll
      for (int u = 1; u < 8; ++u) m = fmaxf(m, S[u]);
      m = fmaxf(m, dppf<XOR1>(m)); m = fmaxf(m, dppf<XOR2>(m)); m = xrow_max(m);
      const float mc = m * LOG2E;
      float l = 0.f;
#pragma unroll
      for (int u = 0; u < 8; ++u) {
        const float p = __builtin_amdgcn_exp2f(fmaf(S[u], LOG2E, -mc));
        pp[u] = cvtpk(p, p);
        l += bflo(pp[u]);
      }
      l += dppf<XOR1>(l); l += dppf<XOR2>(l); l = xrow_sum(l);
      if ((lane & 3) == 0 && lg == 0) {
        *(LAS float*)(lds + L_MX + ((li >> 2) * 8 + wave) * 4) = m;
        *(LAS float*)(lds + L_LW + ((li >> 2) * 8 + wave) * 4) = l;
      }
      wait_lgkm();
      if (lane == 0) __hip_atomic_fetch_add((LAS u32*)(lds + L_CTL + 4 * C_ML), 1u, __ATOMIC_RELAXED, __HIP_MEMORY_SCOPE_WORKGROUP);
    };
    auto consume_v = [&](auto vc_) {
      constexpr int v = decltype(vc_)::value, u = v >> 2, t = v & 3;
      const u32 pb = dppu<t * 0x55>(pp[u]);
      const u32x2 a = {pb & mask0, pb & mask1};
      acc0 = mfma16(a, u32x2{V[v][0], V[v][1]}, acc0);
      acc1 = mfma16(a, u32x2{V[v][2], V[v][3]}, acc1);
    };
    auto perm_wo = [&](auto ksc) {
      constexpr int ks = decltype(ksc)::value;
#pragma unroll
      for (int c = 0; c < 8; ++c)
#pragma unroll
        for (int d = 0; d < 4; ++d)
          WB[ks][c][d] = __builtin_amdgcn_perm(WO[ks * 8 + 2 * d + 1][c >> 1], WO[ks * 8 + 2 * d][c >> 1], (c & 1) ? 0x07060302u : 0x05040100u);
    };
    auto consume = [&](auto ic) {
      constexpr int i = decltype(ic)::value;
      if constexpr (i < 32) {
        if constexpr ((i & 3) == 3) consume_kgroup(IC(i >> 2));
        if constexpr (i == 31) { STAMP(3); softmax(); STAMP(4); }
      } else {
        constexpr int m = i - 32, grp = m / 3, r = m % 3;
        if constexpr (r < 2) consume_v(IC(grp * 2 + r));
        else if constexpr (grp == 7) perm_wo(IC(0));
      }
    };
    for_range<PARK, NLD>([&](auto nc) {
      constexpr int n = decltype(nc)::value;
      for_range<(n == PARK ? 0 : cons_target(n - 1)), cons_target(n)>([&](auto i) { consume(i); });
      SB(); wait_vm<WIN - 1>(); SB(); issue(nc); SB();
    });
    for_range<cons_target(NLD - 1), NLD>([&](auto i) { consume(i); SB(); });
    STAMP(5);
    *(LAS f32x4*)(lds + L_RED + wave * 2048 + lane * 32) = acc0;
    *(LAS f32x4*)(lds + L_RED + wave * 2048 + lane * 32 + 16) = acc1;
    bar();
    if (wave < 4) {
      const f32x4 fa = *(const LAS f32x4*)(lds + L_FS + wave * 32), fb = *(const LAS f32x4*)(lds + L_FS + wave * 32 + 16);
      f32x2 s = *(const LAS f32x2*)(lds + L_RED + tid * 8) * fa[0];
#pragma unroll
      for (int w = 1; w < 8; ++w) s += *(const LAS f32x2*)(lds + L_RED + w * 2048 + tid * 8) * (w < 4 ? fa[w & 3] : fb[w & 3]);
      st_gran(ws, WS_PA + (grp_off * 4 + sp) * 2048 + tid * 8, tag, cvtpk(s[0], s[1]));
    }
    SB();
    perm_wo(IC(1));
    SB();
    STAMP(6);
    u32x4 A[2];
    {
      const u32 off = WS_PA + (grp_off * 4 + (li & 3)) * 2048 + (32 * wave + 4 * lg) * 8 + (li >> 3) * 128 + ((li >> 2) & 1) * 16;
      u32x4 g;
      bool ok = false;
      for (u32 spins = 0;; ++spins) {
        if (!ok) {
          g = ld_gran2(rs, off);
          ok = g[1] == tag && g[3] == tag;
        }
        if (__all(ok) || spins > SPIN_CAP) break;
        __builtin_amdgcn_s_sleep(ASLEEP);
      }
      A[0] = u32x4{g[0], g[2], dppu<0x104>(g[0]), dppu<0x104>(g[2])};
      A[1] = u32x4{dppu<0x108>(g[0]), dppu<0x108>(g[2]), dppu<0x10C>(g[0]), dppu<0x10C>(g[2])};
    }
    STAMP(7);
    {
      float o[8];
#pragma unroll
      for (int c = 0; c < 8; ++c) {
        f32x4 d = {0.f, 0.f, 0.f, 0.f};
        d = mfma32(A[0], WB[0][c], d);
        d = mfma32(A[1], WB[1][c], d);
        o[c] = (d[0] + d[1]) + (d[2] + d[3]);
      }
      if (lg == 0) {
        *(LAS f32x4*)(lds + L_RED2 + wave * 512 + li * 32) = f32x4{o[0], o[1], o[2], o[3]};
        *(LAS f32x4*)(lds + L_RED2 + wave * 512 + li * 32 + 16) = f32x4{o[4], o[5], o[6], o[7]};
      }
    }
    bar();
    if (wave < 2) {
      float s = *(const LAS float*)(lds + L_RED2 + tid * 4);
#pragma unroll
      for (int w = 1; w < 8; ++w) s += *(const LAS float*)(lds + L_RED2 + w * 512 + tid * 4);
      st_gran(ws, WS_PB + grp_off * 4096 + (128 * sp + tid) * 8, tag, __float_as_uint(s));
    }
    STAMP(8);
    if (!final_wg) return;
    const float xv = bflo(x[b * DM + tid]);
    float a;
    for (u32 spins = 0;; ++spins) {
      u64 g[8];
#pragma unroll
      for (int k = 0; k < 8; ++k) g[k] = ld_gran(ws, WS_PB + (b * HKV + k) * 4096 + tid * 8);
      bool ok = true;
      a = 0.f;
#pragma unroll
      for (int k = 0; k < 8; ++k) { ok &= (u32)(g[k] >> 32) == tag; a += __uint_as_float((u32)g[k]); }
      if (__all(ok) || spins > SPIN_CAP) break;
      __builtin_amdgcn_s_sleep(1);
    }
    STAMP(9);
    const float hv = rbf(xv + rbf(a));
    *(LAS float*)(lds + L_HROW + tid * 4) = hv;
    bar();
    float tv;
    {
      const f32x4 h0 = *(const LAS f32x4*)(lds + L_HROW + lane * 32), h1 = *(const LAS f32x4*)(lds + L_HROW + lane * 32 + 16);
      const float piv = *(const LAS float*)(lds + L_HROW);
      float v[8];
#pragma unroll
      for (int e = 0; e < 4; ++e) { v[e] = h0[e]; v[4 + e] = h1[e]; }
      float s1 = 0.f, q1 = 0.f;
#pragma unroll
      for (int e = 0; e < 8; ++e) { const float d = v[e] - piv; s1 += d; q1 = fmaf(d, d, q1); }
      s1 = wave_sum(s1) * (1.0f / DM); q1 = wave_sum(q1) * (1.0f / DM);
      const float mb1 = rbf(piv + s1);
      const float inv1 = __builtin_amdgcn_rcpf(rbf(__builtin_amdgcn_sqrtf(rbf(rbf(fmaxf(fmaf(-s1, s1, q1), 0.f)) + 1e-5f))));
      float s2 = 0.f, q2 = 0.f;
#pragma unroll
      for (int e = 0; e < 8; e += 2) {
        const u32 d = cvtpk(v[e] - mb1, v[e + 1] - mb1);
        const u32 n = cvtpk(bflo(d) * inv1, bfhi(d) * inv1);
        const float n0 = bflo(n), n1 = bfhi(n);
        s2 += n0 + n1; q2 = fmaf(n0, n0, q2); q2 = fmaf(n1, n1, q2);
      }
      const float t1 = rbf(rbf(hv - mb1) * inv1);
      s2 = wave_sum(s2) * (1.0f / DM); q2 = wave_sum(q2) * (1.0f / DM);
      const float inv2 = __builtin_amdgcn_rcpf(rbf(__builtin_amdgcn_sqrtf(rbf(rbf(fmaxf(fmaf(-s2, s2, q2), 0.f)) + 1e-5f))));
      tv = rbf(rbf(t1 - rbf(s2)) * inv2);
    }
    const u32 bits = __float_as_uint(tv) >> 16;
    const u32 rel = (bits & 0x7fffu) - (E_LO << 7);
    const bool in = rel < (u32)NT_HALF;
    float y = *(const LAS float*)(lds + L_TAB + ((in ? rel : 0u) + (bits >> 15) * NT_HALF) * 4);
    u64 mm = __ballot(!in);
    while (mm) {
      const int src = __builtin_ctzll(mm);
      mm &= mm - 1;
      const float ts = __builtin_bit_cast(float, __builtin_amdgcn_readlane(__float_as_uint(tv), src));
      float acc = 0.f;
#pragma unroll 1
      for (int f = lane; f < FF; f += 64) acc = gelu_term(ts, bflo(w1[f]), bflo(b1[f]), bflo(w2[f]), acc);
      acc = wave_sum(acc);
      if (lane == src) y = rbf(rbf(acc) + bflo(b2[0]));
    }
    out[b * DM + tid] = (u16)cvtpk(hv + y, 0.f);
    if (b == 0 && tid == 0) __hip_atomic_store((GAS u32*)(ws + WS_EPOCH), tag, RLX_AGENT);
    STAMP(10);
    return;
  }

  {
    const int tw = hwave - NSW - 1;
    __builtin_amdgcn_s_setprio(3);
    bar();
    SB();
    const u32 tag = *(const CAS u32*)(ws + WS_EPOCH) + 1u;
    u32 pf = 0;
    if (tw == 0) {
      u64 pc; u32 back;
      asm volatile("s_getpc_b64 %0\n.Lpf_here:\n\ts_mov_b32 %1, .Lpf_here-fused_kernel" : "=s"(pc), "=s"(back));
      const u32 o = ((wg >> 3) * 8 + lane) * 128;
      if (lane < 8 && o + 4 <= CODE_BYTES) pf = *(const GAS u32*)(pc - back + o);
    }
    __builtin_amdgcn_s_sleep(TSLEEP);
    asm volatile("" ::: "memory");
    u32x2 r1[7], rb[7], r2[7];
#pragma unroll
    for (int c = 0; c < 7; ++c) {
      r1[c] = ((const u32x2*)w1)[c * 64 + lane];
      rb[c] = ((const u32x2*)b1)[c * 64 + lane];
      r2[c] = ((const u32x2*)w2)[c * 64 + lane];
    }
    const float b2v = bflo(b2[0]);
    const int idx0 = wg * 24 + tw * 8;
    f32x2 f1[14], fb[14], f2[14];
#pragma unroll
    for (int c = 0; c < 14; ++c) {
      f1[c] = f32x2{bflo(r1[c >> 1][c & 1]), bfhi(r1[c >> 1][c & 1])};
      fb[c] = f32x2{bflo(rb[c >> 1][c & 1]), bfhi(rb[c >> 1][c & 1])};
      f2[c] = f32x2{bflo(r2[c >> 1][c & 1]), bfhi(r2[c >> 1][c & 1])};
    }
    __builtin_amdgcn_s_setprio(0);
    float res = 0.f;
#pragma unroll 1
    for (int e = 0; e < 8; ++e) {
      const int i0 = idx0 + e;
      const float t0 = __uint_as_float((u32)((i0 >= NT_HALF ? i0 - NT_HALF + 0x8000 : i0) + (E_LO << 7)) << 16);
      const f32x2 tt = {t0, t0};
      f32x2 acc = {0.f, 0.f};
#pragma unroll
      for (int c0_ = 0; c0_ < 14; c0_ += 7) {
        f32x2 u[7], z[7];
#pragma unroll
        for (int c = 0; c < 7; ++c) u[c] = __builtin_elementwise_fma(tt, f1[c0_ + c], fb[c0_ + c]);
        SB();
#pragma unroll
        for (int c = 0; c < 7; ++c) z[c] = u[c] * u[c];
        SB();
#pragma unroll
        for (int c = 0; c < 7; ++c) z[c] = __builtin_elementwise_fma(z[c], f32x2{GK1, GK1}, f32x2{GK0, GK0});
        SB();
#pragma unroll
        for (int c = 0; c < 7; ++c) z[c] = z[c] * u[c];
        SB();
#pragma unroll
        for (int c = 0; c < 7; ++c) z[c] = f32x2{__builtin_amdgcn_exp2f(z[c][0]), __builtin_amdgcn_exp2f(z[c][1])};
        SB();
#pragma unroll
        for (int c = 0; c < 7; ++c) { z[c] = z[c] + f32x2{1.f, 1.f}; u[c] = u[c] * f2[c0_ + c]; }
        SB();
#pragma unroll
        for (int c = 0; c < 7; ++c) z[c] = f32x2{__builtin_amdgcn_rcpf(z[c][0]), __builtin_amdgcn_rcpf(z[c][1])};
        SB();
#pragma unroll
        for (int c = 0; c < 7; ++c) acc = __builtin_elementwise_fma(u[c], z[c], acc);
        SB();
      }
      const float a0 = wave_sum(acc[0] + acc[1]);
      if (lane == e) res = a0;
    }
    __builtin_amdgcn_s_setprio(3);
    if (lane < 8) st_gran(ws, WS_TB + (idx0 + lane) * 8, tag, __float_as_uint(rbf(rbf(res) + b2v)));
    asm volatile("" ::"v"(pf));
    STAMPW();
    STAMP(1);
    if (final_wg) {
      lds_wait_ge<8>(ctl + C_ML, NSW);
      const u32 base = (u32)tw * 1024 + lane;
#pragma unroll 1
      for (int half = 0; half < 2; ++half) {
        for (u32 spins = 0;; ++spins) {
          u32x4 g[8];
          bool ok = true;
#pragma unroll
          for (int k = 0; k < 8; ++k) g[k] = ld_gran2(rs, WS_TB + (base + (half * 8 + k) * 64) * 16);
#pragma unroll
          for (int k = 0; k < 8; ++k) {
            ok &= g[k][1] == tag && g[k][3] == tag;
            *(LAS u32x2*)(lds + L_TAB + (base + (half * 8 + k) * 64) * 8) = u32x2{g[k][0], g[k][2]};
          }
          if (__all(ok) || spins > SPIN_CAP) break;
          __builtin_amdgcn_s_sleep(8);
        }
      }
      wait_lgkm();
    }
    STAMP(2);
  }
}

extern "C" void kernel_launch(void* const* d_in, const int* in_sizes, int n_in, void* d_out, int out_size, void* d_ws, size_t ws_size,
                              hipStream_t stream) {
  fused_kernel<<<dim3(NWG), dim3(NTHR), 0, stream>>>((const u16*)d_in[0], (const u16*)d_in[1], (const u16*)d_in[2], (const u16*)d_in[3],
                                                     (const u16*)d_in[4], (const u16*)d_in[6], (const u16*)d_in[7], (const u16*)d_in[8],
                                                     (const u16*)d_in[9], (u16*)d_out, (unsigned char*)d_ws);
}
```

```cpp
#include <hip/hip_runtime.h>
#include <stdint.h>
#include <utility>
#include <type_traits>

typedef unsigned short u16;
typedef unsigned u32;
typedef unsigned long long u64;
typedef u32 u32x2 __attribute__((ext_vector_type(2)));
typedef u32 u32x4 __attribute__((ext_vector_type(4)));
typedef float f32x2 __attribute__((ext_vector_type(2)));
typedef float f32x4 __attribute__((ext_vector_type(4)));
typedef short s16x4 __attribute__((ext_vector_type(4)));
typedef __bf16 bf16x8 __attribute__((ext_vector_type(8)));
#define LAS __attribute__((address_space(3)))
#define GAS __attribute__((address_space(1)))
#define CAS __attribute__((address_space(4)))
#define RLX_AGENT __ATOMIC_RELAXED, __HIP_MEMORY_SCOPE_AGENT

#define K_NT 1
#define V_NT 1
#define W_NT 0
#define TLB_PF 1
#define WIN 6
#define QTRIG 14
#define MLTRIG 62
#define TSLEEP 100
#define CODE_PF 1
#define CODE_BYTES 17500
#define CRATE 3
#define ASLEEP 2

constexpr int DM = 512, HKV = 8, SK = 4096, FF = 1792;
constexpr int NWG = 256, NSW = 8, NTHR = 768, PARK = 32, NLD = 80;
constexpr int NE = 24, E_LO = 132 - NE, NT_HALF = NE * 128, NTAB = 2 * NT_HALF;
static_assert(NTAB == NWG * 24, "24 table entries per workgroup");
constexpr float SCALE = 0.08838834764831845f, LOG2E = 1.4426950408889634f;
constexpr float GK0 = -2.302208198f, GK1 = GK0 * 0.044715f;
constexpr u32 SPIN_CAP = 1u << 15, LSPIN_CAP = 1u << 19;

constexpr u32 WS_EPOCH = 0, WS_QG = 4096, WS_ML = WS_QG + 1048576, WS_PA = WS_ML + 16384, WS_PB = WS_PA + 524288, WS_TB = WS_PB + 262144,
              WS_END = WS_TB + NTAB * 8, WS_DBG = 2u << 20;
constexpr u32 L_QF = 0, L_CTL = 1024, L_MX = 1088, L_LW = 1216, L_FS = 1344, L_RED2 = 2048, L_HROW = L_RED2 + 4096, L_RED = L_HROW + 2048,
              L_TAB = L_RED + 16384, L_WQ = L_TAB + NTAB * 4, L_END = L_WQ + 16384;
enum { C_Q = 0, C_ML = 1, C_QTRIG = 2, C_MLTRIG = 3, C_TAG = 4 };

__device__ __forceinline__ float bflo(u32 w) { return __builtin_bit_cast(float, w << 16); }
__device__ __forceinline__ float bfhi(u32 w) { return __builtin_bit_cast(float, w & 0xffff0000u); }
__device__ __forceinline__ u32 cvtpk(float lo, float hi) {
  typedef __bf16 bf16x2 __attribute__((ext_vector_type(2)));
  bf16x2 r = {(__bf16)lo, (__bf16)hi};
  return __builtin_bit_cast(u32, r);
}
__device__ __forceinline__ float rbf(float f) { return bflo(cvtpk(f, f)); }
template <int CTRL> __device__ __forceinline__ float dppf(float x) {
  return __builtin_bit_cast(float, __builtin_amdgcn_mov_dpp(__builtin_bit_cast(int, x), CTRL, 0xf, 0xf, true));
}
template <int CTRL> __device__ __forceinline__ u32 dppu(u32 x) { return (u32)__builtin_amdgcn_mov_dpp((int)x, CTRL, 0xf, 0xf, true); }
constexpr int XOR1 = 0xB1, XOR2 = 0x4E, HMIR = 0x141, MIR = 0x140, ROR8 = 0x128;
__device__ __forceinline__ float xrow_sum(float x) {
  auto s = __builtin_amdgcn_permlane16_swap(__float_as_uint(x), __float_as_uint(x), false, false);
  x = __uint_as_float(s[0]) + __uint_as_float(s[1]);
  auto t = __builtin_amdgcn_permlane32_swap(__float_as_uint(x), __float_as_uint(x), false, false);
  return __uint_as_float(t[0]) + __uint_as_float(t[1]);
}
__device__ __forceinline__ float xrow_max(float x) {
  auto s = __builtin_amdgcn_permlane16_swap(__float_as_uint(x), __float_as_uint(x), false, false);
  x = fmaxf(__uint_as_float(s[0]), __uint_as_float(s[1]));
  auto t = __builtin_amdgcn_permlane32_swap(__float_as_uint(x), __float_as_uint(x), false, false);
  return fmaxf(__uint_as_float(t[0]), __uint_as_float(t[1]));
}
__device__ __forceinline__ float wave_sum(float x) {
  x += dppf<XOR1>(x); x += dppf<XOR2>(x); x += dppf<HMIR>(x); x += dppf<MIR>(x);
  return xrow_sum(x);
}
__device__ __forceinline__ f32x4 mfma32(u32x4 a, u32x4 b, f32x4 c) {
  return __builtin_amdgcn_mfma_f32_16x16x32_bf16(__builtin_bit_cast(bf16x8, a), __builtin_bit_cast(bf16x8, b), c, 0, 0, 0);
}
__device__ __forceinline__ f32x4 mfma16(u32x2 a, u32x2 b, f32x4 c) {
  return __builtin_amdgcn_mfma_f32_16x16x16bf16_1k(__builtin_bit_cast(s16x4, a), __builtin_bit_cast(s16x4, b), c, 0, 0, 0);
}
template <int N> __device__ __forceinline__ void wait_vm() { asm volatile("s_waitcnt vmcnt(%0)" ::"n"(N) : "memory"); }
__device__ __forceinline__ void wait_lgkm() { asm volatile("s_waitcnt lgkmcnt(0)" ::: "memory"); }
__device__ __forceinline__ void bar() { asm volatile("s_waitcnt lgkmcnt(0)\n\ts_barrier" ::: "memory"); }
#define SB() __builtin_amdgcn_sched_barrier(0)
#define IC(n) std::integral_constant<int, (n)>{}
template <int L, typename F, int... I> __device__ __forceinline__ void for_impl(F&& f, std::integer_sequence<int, I...>) { (f(std::integral_constant<int, L + I>{}), ...); }
template <int L, int H, typename F> __device__ __forceinline__ void for_range(F&& f) { for_impl<L>(f, std::make_integer_sequence<int, (H > L ? H - L : 0)>{}); }
template <int NT> __device__ __forceinline__ u32x4 ldp(const u32x4* p) { if constexpr (NT) return __builtin_nontemporal_load(p); else return *p; }
__device__ __forceinline__ void st_gran(unsigned char* ws, u32 off, u32 tag, u32 val) {
  __hip_atomic_store((GAS u64*)(ws + off), ((u64)tag << 32) | val, RLX_AGENT);
}
__device__ __forceinline__ u64 ld_gran(unsigned char* ws, u32 off) { return __hip_atomic_load((GAS u64*)(ws + off), RLX_AGENT); }
__device__ __forceinline__ u32x4 ld_gran2(__amdgpu_buffer_rsrc_t rs, u32 off) { return __builtin_amdgcn_raw_buffer_load_b128(rs, off, 0, 16); }
template <int SL> __device__ __forceinline__ void lds_wait_ge(volatile LAS u32* w, u32 n) {
  for (u32 spins = 0; *w < n && spins < LSPIN_CAP; ++spins) __builtin_amdgcn_s_sleep(SL);
  asm volatile("" ::: "memory");
}

#define STAMPR(i) do {} while (0)
#define STAMPW() do {} while (0)
#define STAMP(i) do {} while (0)

__device__ __forceinline__ float gelu_term(float t, float w1, float b1, float w2, float acc) {
  const float u = fmaf(t, w1, b1);
  const float z = u * fmaf(u * u, GK1, GK0);
  const float e = __builtin_amdgcn_exp2f(z);
  return fmaf(u * w2, __builtin_amdgcn_rcpf(1.0f + e), acc);
}
constexpr int cons_target(int n) { return (n - WIN + 1) < (4 + CRATE * (n - PARK)) ? (n - WIN + 1) : (4 + CRATE * (n - PARK)); }

extern "C" __global__ void __launch_bounds__(NTHR) fused_kernel(const u16* x, const u16* kc, const u16* vc, const u16* wq, const u16* wo,
                                                                const u16* w1, const u16* b1, const u16* w2, const u16* b2, u16* out,
                                                                unsigned char* ws) {
  __shared__ __attribute__((aligned(16))) unsigned char lds[L_END];
  const int lane = threadIdx.x & 63, hwave = __builtin_amdgcn_readfirstlane(threadIdx.x >> 6);
  const int wg = blockIdx.x, kvh = wg & 7, sp = (wg >> 3) & 3, b = wg >> 5;
  const bool final_wg = (kvh == b) && (sp == 0);
  const int li = lane & 15, lg = lane >> 4;
  volatile LAS u32* ctl = (volatile LAS u32*)(lds + L_CTL);
  const __amdgpu_buffer_rsrc_t rs = __builtin_amdgcn_make_buffer_rsrc(ws, 0, WS_END, 0x00020000);
  const u32 grp_off = (u32)(b * HKV + kvh);

  if (hwave == 0) {
    __builtin_amdgcn_s_setprio(3);
    if (lane < 4) ctl[lane] = 0;
    bar();
    SB();
    const int cb = (wg >> 3) & 7, kq = wg >> 6;
    u32x4 xa[8], wt[16];
    const u32x4* xp = (const u32x4*)x + (li & 7) * 64 + (li >> 3) * 4 + lg;
    const u32x4* wp = (const u32x4*)(wq + (size_t)(128 * kq) * 4096 + kvh * 512 + 64 * cb) + (lane >> 3) * 512 + (lane & 7);
#pragma unroll
    for (int k = 0; k < 8; ++k) xa[k] = xp[((k + 2 * kq) & 7) * 8];
#pragma unroll
    for (int k = 0; k < 16; ++k) wt[k] = ldp<W_NT>(wp + k * 8 * 512);
    const u32 tag = *(const CAS u32*)(ws + WS_EPOCH) + 1u;
    SB();
    STAMPR(0);
    {
      float sm = 0.f;
#pragma unroll
      for (int k = 0; k < 8; ++k)
#pragma unroll
        for (int e = 0; e < 4; ++e) sm += bflo(xa[k][e]) + bfhi(xa[k][e]);
      sm += dppf<ROR8>(sm);
      const float mean = xrow_sum(sm) * (1.0f / DM);
      float sq = 0.f;
#pragma unroll
      for (int k = 0; k < 8; ++k)
#pragma unroll
        for (int e = 0; e < 4; ++e) { const float d0 = bflo(xa[k][e]) - mean, d1 = bfhi(xa[k][e]) - mean; sq = fmaf(d0, d0, sq); sq = fmaf(d1, d1, sq); }
      sq += dppf<ROR8>(sq);
      const float var = rbf(xrow_sum(sq) * (1.0f / DM)), mb = rbf(mean);
      const float inv = __builtin_amdgcn_rcpf(rbf(__builtin_amdgcn_sqrtf(rbf(var + 1e-5f))));
#pragma unroll
      for (int k = 0; k < 2; ++k)
#pragma unroll
        for (int e = 0; e < 4; ++e) {
          const u32 d = cvtpk(bflo(xa[k][e]) - mb, bfhi(xa[k][e]) - mb);
          xa[k][e] = cvtpk(bflo(d) * inv, bfhi(d) * inv);
        }
    }
    SB();
    STAMPR(1);
#pragma unroll
    for (int k = 0; k < 16; ++k) *(LAS u32x4*)(lds + L_WQ + k * 1024 + lane * 16) = wt[k];
    wait_lgkm();
    STAMPR(2);
    const u32 troff = L_WQ + (8 * lg + (li >> 2)) * 128 + (li & 3) * 8;
    const bool odd = lane & 1, up = lg & 2;
    const u32 qoff = WS_QG + ((4 * (lg & 1) + (lg & 2) + (lane & 1)) * HKV + kvh) * 16384 + kq * 4096 + (64 * cb + (li & 14)) * 8;
#pragma unroll
    for (int ct = 0; ct < 4; ++ct) {
      f32x4 d0 = {0.f, 0.f, 0.f, 0.f}, d1 = {0.f, 0.f, 0.f, 0.f};
#pragma unroll
      for (int k = 0; k < 4; ++k) {
        const s16x4 b0 = __builtin_amdgcn_ds_read_tr16_b64_v4i16((LAS s16x4*)(lds + troff + k * 4096 + ct * 32));
        const s16x4 b1_ = __builtin_amdgcn_ds_read_tr16_b64_v4i16((LAS s16x4*)(lds + troff + k * 4096 + ct * 32 + 512));
        const u32x2 p0 = __builtin_bit_cast(u32x2, b0), p1 = __builtin_bit_cast(u32x2, b1_);
        if (k & 1) d1 = mfma32(xa[k >> 1], u32x4{p0[0], p0[1], p1[0], p1[1]}, d1);
        else d0 = mfma32(xa[k >> 1], u32x4{p0[0], p0[1], p1[0], p1[1]}, d0);
      }
      const auto s0 = __builtin_amdgcn_permlane32_swap(__float_as_uint(d1[0]), __float_as_uint(d0[2]), false, false);
      const auto s1 = __builtin_amdgcn_permlane32_swap(__float_as_uint(d1[1]), __float_as_uint(d0[3]), false, false);
      const float e0 = up ? d1[2] + __uint_as_float(s0[0]) : d0[0] + __uint_as_float(s0[1]);
      const float e1 = up ? d1[3] + __uint_as_float(s1[0]) : d0[1] + __uint_as_float(s1[1]);
      const float mine = odd ? e1 : e0, got = dppf<XOR1>(odd ? e0 : e1);
      const u32x4 g = {__float_as_uint(odd ? got : mine), tag, __float_as_uint(odd ? mine : got), tag};
      __builtin_amdgcn_raw_buffer_store_b128(g, rs, qoff + ct * 128, 0, 16);
    }
    STAMPR(3);
    STAMPW();
    lds_wait_ge<2>(ctl + C_QTRIG, 1);
    STAMP(1);
    {
      const u32 off = WS_QG + grp_off * 16384 + lane * 16;
      for (u32 spins = 0;; ++spins) {
        u32x4 g[16];
#pragma unroll
        for (int k = 0; k < 16; ++k) g[k] = ld_gran2(rs, off + k * 1024);
        bool ok = true;
#pragma unroll
        for (int k = 0; k < 16; ++k) ok &= g[k][1] == tag && g[k][3] == tag;
#pragma unroll
        for (int h = 0; h < 4; ++h) {
          const float q0 = (__uint_as_float(g[h][0]) + __uint_as_float(g[4 + h][0])) + (__uint_as_float(g[8 + h][0]) + __uint_as_float(g[12 + h][0]));
          const float q1 = (__uint_as_float(g[h][2]) + __uint_as_float(g[4 + h][2])) + (__uint_as_float(g[8 + h][2]) + __uint_as_float(g[12 + h][2]));
          *(LAS u32*)(lds + L_QF + (h * 64 + lane) * 4) = cvtpk(q0, q1);
        }
        if (__all(ok) || spins > SPIN_CAP) break;
        __builtin_amdgcn_s_sleep(8);
      }
      if (lane == 0) ctl[C_TAG] = tag;
      wait_lgkm();
      if (lane == 0) ctl[C_Q] = 1;
    }
    STAMP(2);
    lds_wait_ge<4>(ctl + C_ML, NSW);
    {
      const float mw = *(const LAS float*)(lds + L_MX + (lane & 31) * 4), lw = *(const LAS float*)(lds + L_LW + (lane & 31) * 4);
      float M = fmaxf(mw, dppf<XOR1>(mw)); M = fmaxf(M, dppf<XOR2>(M)); M = fmaxf(M, dppf<HMIR>(M));
      float l = lw * __builtin_amdgcn_exp2f((mw - M) * LOG2E);
      l += dppf<XOR1>(l); l += dppf<XOR2>(l); l += dppf<HMIR>(l);
      const u32 goff = WS_ML + grp_off * 256;
      if (lane < 32 && (lane & 7) < 2) st_gran(ws, goff + sp * 64 + ((lane >> 3) * 2 + (lane & 1)) * 8, tag, __float_as_uint((lane & 1) ? l : M));
      STAMP(3);
      lds_wait_ge<4>(ctl + C_MLTRIG, 1);
      u32x4 g;
      for (u32 spins = 0;; ++spins) {
        g = ld_gran2(rs, goff + (lane & 3) * 64 + ((lane >> 3) & 3) * 16);
        if (__all(g[1] == tag && g[3] == tag) || spins > SPIN_CAP) break;
        __builtin_amdgcn_s_sleep(8);
      }
      const float Mp = __uint_as_float(g[0]);
      float Ms = fmaxf(Mp, dppf<XOR1>(Mp)); Ms = fmaxf(Ms, dppf<XOR2>(Ms));
      float L = __uint_as_float(g[2]) * __builtin_amdgcn_exp2f((Mp - Ms) * LOG2E);
      L += dppf<XOR1>(L); L += dppf<XOR2>(L);
      if (lane < 32) *(LAS float*)(lds + L_FS + lane * 4) = __builtin_amdgcn_exp2f((mw - Ms) * LOG2E) / L;
    }
    STAMP(4);
    bar();
    return;
  }

  if (hwave <= NSW) {
    const int wave = hwave - 1, tid = threadIdx.x - 64;
    bar();
    SB();
    const size_t tile = ((size_t)grp_off * SK + sp * 1024) * 16;
    const u32x4* kb = (const u32x4*)kc + tile + wave * 256;
    const u32x4* vb = (const u32x4*)vc + tile + wave * 256;
    const u32x4* wb = (const u32x4*)wo + (size_t)(kvh * 512 + 64 * wave) * 64 + 16 * sp;
    const u32 klane = ((lane >> 2) & 3) * 16 + 4 * lg + (lane & 3);
    const u32 wlane = lg * 8 * 64 + li;
    u32x4 K[32], V[32], WO[16];
    auto issue = [&](auto nc) {
      constexpr int n = decltype(nc)::value;
      if constexpr (n < 32) {
        K[n] = ldp<K_NT>(kb + (n >> 2) * 2048 + (n & 3) * 64 + klane);
      } else {
        constexpr int m = n - 32, grp = m / 3, r = m % 3;
        if constexpr (r < 2) { constexpr int v = grp * 2 + r; V[v] = ldp<V_NT>(vb + (v >> 2) * 2048 + (v & 3) * 64 + lane); }
        else { WO[grp] = ldp<W_NT>(wb + ((grp >> 3) * 32 + (grp & 7)) * 64 + wlane); }
      }
      if constexpr (n == QTRIG) ctl[C_QTRIG] = 1;
      if constexpr (n == MLTRIG) ctl[C_MLTRIG] = 1;
    };
    for_range<0, WIN>([&](auto n) { issue(n); });
    SB();
    STAMPR(0);
    for_range<WIN, PARK>([&](auto n) {
      wait_vm<WIN - 1>(); SB();
      if constexpr (decltype(n)::value == WIN) STAMPR(1);
      if constexpr (decltype(n)::value == WIN + 6) STAMPR(2);
      if constexpr (decltype(n)::value == WIN + 12) STAMPR(3);
      issue(n); SB();
    });
    STAMPW();
    STAMP(1);
    lds_wait_ge<1>(ctl + C_Q, 1);
    const u32 tag = __builtin_amdgcn_readfirstlane(ctl[C_TAG]);
    const u32x4 qf = *(const LAS u32x4*)(lds + L_QF + (li >> 2) * 256 + lg * 64 + (li & 3) * 16);
    STAMP(2);
    const bool c0 = lane & 1, c1 = lane & 2;
    float S[8];
    u32 pp[8];
    f32x4 acc0 = {0.f, 0.f, 0.f, 0.f}, acc1 = {0.f, 0.f, 0.f, 0.f};
    const u32 half = c0 ? 0xffff0000u : 0x0000ffffu;
    const u32 mask0 = c1 ? 0u : half, mask1 = c1 ? half : 0u;
    u32x4 WB[2][8];
    auto consume_kgroup = [&](auto uc) {
      constexpr int u = decltype(uc)::value;
      const f32x4 z = {0.f, 0.f, 0.f, 0.f};
      float v[4];
#pragma unroll
      for (int t = 0; t < 4; ++t) {
        const f32x4 d = mfma32(K[4 * u + t], qf, z);
        const float lo = c0 ? d[1] : d[0], hi = c0 ? d[3] : d[2];
        v[t] = c1 ? hi : lo;
      }
      const float w0 = (c0 ? v[1] : v[0]) + dppf<XOR1>(c0 ? v[0] : v[1]);
      const float w1_ = (c0 ? v[3] : v[2]) + dppf<XOR1>(c0 ? v[2] : v[3]);
      S[u] = rbf(rbf((c1 ? w1_ : w0) + dppf<XOR2>(c1 ? w0 : w1_)) * SCALE);
    };
    auto softmax = [&]() {
      float m = S[0];
#pragma unroll
      for (int u = 1; u < 8; ++u) m = fmaxf(m, S[u]);
      m = fmaxf(m, dppf<XOR1>(m)); m = fmaxf(m, dppf<XOR2>(m)); m = xrow_max(m);
      const float mc = m * LOG2E;
      float l = 0.f;
#pragma unroll
      for (int u = 0; u < 8; ++u) {
        const float p = __builtin_amdgcn_exp2f(fmaf(S[u], LOG2E, -mc));
        pp[u] = cvtpk(p, p);
        l += bflo(pp[u]);
      }
      l += dppf<XOR1>(l); l += dppf<XOR2>(l); l = xrow_sum(l);
      if ((lane & 3) == 0 && lg == 0) {
        *(LAS float*)(lds + L_MX + ((li >> 2) * 8 + wave) * 4) = m;
        *(LAS float*)(lds + L_LW + ((li >> 2) * 8 + wave) * 4) = l;
      }
      wait_lgkm();
      if (lane == 0) __hip_atomic_fetch_add((LAS u32*)(lds + L_CTL + 4 * C_ML), 1u, __ATOMIC_RELAXED, __HIP_MEMORY_SCOPE_WORKGROUP);
    };
    auto consume_v = [&](auto vc_) {
      constexpr int v = decltype(vc_)::value, u = v >> 2, t = v & 3;
      const u32 pb = dppu<t * 0x55>(pp[u]);
      const u32x2 a = {pb & mask0, pb & mask1};
      acc0 = mfma16(a, u32x2{V[v][0], V[v][1]}, acc0);
      acc1 = mfma16(a, u32x2{V[v][2], V[v][3]}, acc1);
    };
    auto perm_wo = [&](auto ksc) {
      constexpr int ks = decltype(ksc)::value;
#pragma unroll
      for (int c = 0; c < 8; ++c)
#pragma unroll
        for (int d = 0; d < 4; ++d)
          WB[ks][c][d] = __builtin_amdgcn_perm(WO[ks * 8 + 2 * d + 1][c >> 1], WO[ks * 8 + 2 * d][c >> 1], (c & 1) ? 0x07060302u : 0x05040100u);
    };
    auto consume = [&](auto ic) {
      constexpr int i = decltype(ic)::value;
      if constexpr (i < 32) {
        if constexpr ((i & 3) == 3) consume_kgroup(IC(i >> 2));
        if constexpr (i == 31) { STAMP(3); softmax(); STAMP(4); }
      } else {
        constexpr int m = i - 32, grp = m / 3, r = m % 3;
        if constexpr (r < 2) consume_v(IC(grp * 2 + r));
        else if constexpr (grp == 7) perm_wo(IC(0));
      }
    };
    for_range<PARK, NLD>([&](auto nc) {
      constexpr int n = decltype(nc)::value;
      for_range<(n == PARK ? 0 : cons_target(n - 1)), cons_target(n)>([&](auto i) { consume(i); });
      SB(); wait_vm<WIN - 1>(); SB(); issue(nc); SB();
    });
    for_range<cons_target(NLD - 1), NLD>([&](auto i) { consume(i); SB(); });
    STAMP(5);
    *(LAS f32x4*)(lds + L_RED + wave * 2048 + lane * 32) = acc0;
    *(LAS f32x4*)(lds + L_RED + wave * 2048 + lane * 32 + 16) = acc1;
    bar();
    if (wave < 4) {
      const f32x4 fa = *(const LAS f32x4*)(lds + L_FS + wave * 32), fb = *(const LAS f32x4*)(lds + L_FS + wave * 32 + 16);
      f32x2 s = *(const LAS f32x2*)(lds + L_RED + tid * 8) * fa[0];
#pragma unroll
      for (int w = 1; w < 8; ++w) s += *(const LAS f32x2*)(lds + L_RED + w * 2048 + tid * 8) * (w < 4 ? fa[w & 3] : fb[w & 3]);
      st_gran(ws, WS_PA + (grp_off * 4 + sp) * 2048 + tid * 8, tag, cvtpk(s[0], s[1]));
    }
    SB();
    perm_wo(IC(1));
    SB();
    STAMP(6);
    u32x4 A[2];
    {
      const u32 off = WS_PA + (grp_off * 4 + (li & 3)) * 2048 + (32 * wave + 4 * lg) * 8 + (li >> 3) * 128 + ((li >> 2) & 1) * 16;
      u32x4 g;
      bool ok = false;
      for (u32 spins = 0;; ++spins) {
        if (!ok) {
          g = ld_gran2(rs, off);
          ok = g[1] == tag && g[3] == tag;
        }
        if (__all(ok) || spins > SPIN_CAP) break;
        __builtin_amdgcn_s_sleep(ASLEEP);
      }
      A[0] = u32x4{g[0], g[2], dppu<0x104>(g[0]), dppu<0x104>(g[2])};
      A[1] = u32x4{dppu<0x108>(g[0]), dppu<0x108>(g[2]), dppu<0x10C>(g[0]), dppu<0x10C>(g[2])};
    }
    STAMP(7);
    {
      float o[8];
#pragma unroll
      for (int c = 0; c < 8; ++c) {
        f32x4 d = {0.f, 0.f, 0.f, 0.f};
        d = mfma32(A[0], WB[0][c], d);
        d = mfma32(A[1], WB[1][c], d);
        o[c] = (d[0] + d[1]) + (d[2] + d[3]);
      }
      if (lg == 0) {
        *(LAS f32x4*)(lds + L_RED2 + wave * 512 + li * 32) = f32x4{o[0], o[1], o[2], o[3]};
        *(LAS f32x4*)(lds + L_RED2 + wave * 512 + li * 32 + 16) = f32x4{o[4], o[5], o[6], o[7]};
      }
    }
    bar();
    if (wave < 2) {
      float s = *(const LAS float*)(lds + L_RED2 + tid * 4);
#pragma unroll
      for (int w = 1; w < 8; ++w) s += *(const LAS float*)(lds + L_RED2 + w * 512 + tid * 4);
      st_gran(ws, WS_PB + grp_off * 4096 + (128 * sp + tid) * 8, tag, __float_as_uint(s));
    }
    STAMP(8);
    if (!final_wg) return;
    const float xv = bflo(x[b * DM + tid]);
    float a;
    for (u32 spins = 0;; ++spins) {
      u64 g[8];
#pragma unroll
      for (int k = 0; k < 8; ++k) g[k] = ld_gran(ws, WS_PB + (b * HKV + k) * 4096 + tid * 8);
      bool ok = true;
      a = 0.f;
#pragma unroll
      for (int k = 0; k < 8; ++k) { ok &= (u32)(g[k] >> 32) == tag; a += __uint_as_float((u32)g[k]); }
      if (__all(ok) || spins > SPIN_CAP) break;
      __builtin_amdgcn_s_sleep(1);
    }
    STAMP(9);
    const float hv = rbf(xv + rbf(a));
    *(LAS float*)(lds + L_HROW + tid * 4) = hv;
    bar();
    float tv;
    {
      const f32x4 h0 = *(const LAS f32x4*)(lds + L_HROW + lane * 32), h1 = *(const LAS f32x4*)(lds + L_HROW + lane * 32 + 16);
      const float piv = *(const LAS float*)(lds + L_HROW);
      float v[8];
#pragma unroll
      for (int e = 0; e < 4; ++e) { v[e] = h0[e]; v[4 + e] = h1[e]; }
      float s1 = 0.f, q1 = 0.f;
#pragma unroll
      for (int e = 0; e < 8; ++e) { const float d = v[e] - piv; s1 += d; q1 = fmaf(d, d, q1); }
      s1 = wave_sum(s1) * (1.0f / DM); q1 = wave_sum(q1) * (1.0f / DM);
      const float mb1 = rbf(piv + s1);
      const float inv1 = __builtin_amdgcn_rcpf(rbf(__builtin_amdgcn_sqrtf(rbf(rbf(fmaxf(fmaf(-s1, s1, q1), 0.f)) + 1e-5f))));
      float s2 = 0.f, q2 = 0.f;
#pragma unroll
      for (int e = 0; e < 8; e += 2) {
        const u32 d = cvtpk(v[e] - mb1, v[e + 1] - mb1);
        const u32 n = cvtpk(bflo(d) * inv1, bfhi(d) * inv1);
        const float n0 = bflo(n), n1 = bfhi(n);
        s2 += n0 + n1; q2 = fmaf(n0, n0, q2); q2 = fmaf(n1, n1, q2);
      }
      const float t1 = rbf(rbf(hv - mb1) * inv1);
      s2 = wave_sum(s2) * (1.0f / DM); q2 = wave_sum(q2) * (1.0f / DM);
      const float inv2 = __builtin_amdgcn_rcpf(rbf(__builtin_amdgcn_sqrtf(rbf(rbf(fmaxf(fmaf(-s2, s2, q2), 0.f)) + 1e-5f))));
      tv = rbf(rbf(t1 - rbf(s2)) * inv2);
    }
    const u32 bits = __float_as_uint(tv) >> 16;
    const u32 rel = (bits & 0x7fffu) - (E_LO << 7);
    const bool in = rel < (u32)NT_HALF;
    float y = *(const LAS float*)(lds + L_TAB + ((in ? rel : 0u) + (bits >> 15) * NT_HALF) * 4);
    u64 mm = __ballot(!in);
    while (mm) {
      const int src = __builtin_ctzll(mm);
      mm &= mm - 1;
      const float ts = __builtin_bit_cast(float, __builtin_amdgcn_readlane(__float_as_uint(tv), src));
      float acc = 0.f;
#pragma unroll 1
      for (int f = lane; f < FF; f += 64) acc = gelu_term(ts, bflo(w1[f]), bflo(b1[f]), bflo(w2[f]), acc);
      acc = wave_sum(acc);
      if (lane == src) y = rbf(rbf(acc) + bflo(b2[0]));
    }
    out[b * DM + tid] = (u16)cvtpk(hv + y, 0.f);
    if (b == 0 && tid == 0) __hip_atomic_store((GAS u32*)(ws + WS_EPOCH), tag, RLX_AGENT);
    STAMP(10);
    return;
  }

  {
    const int tw = hwave - NSW - 1;
    __builtin_amdgcn_s_setprio(3);
    bar();
    SB();
    const u32 tag = *(const CAS u32*)(ws + WS_EPOCH) + 1u;
    u32 pf = 0;
    if (tw == 0) {
      u64 pc; u32 back;
      asm volatile("s_getpc_b64 %0\n.Lpf_here:\n\ts_mov_b32 %1, .Lpf_here-fused_kernel" : "=s"(pc), "=s"(back));
      const u32 o = ((wg >> 3) * 8 + lane) * 128;
      if (lane < 8 && o + 4 <= CODE_BYTES) pf = *(const GAS u32*)(pc - back + o);
    }
    u32 tp = 0;
    if (tw == 1) {
      const size_t tile = ((size_t)grp_off * SK + sp * 1024) * 256;
      tp ^= *(const GAS u32*)((const char*)kc + tile + lane * 4096 + 64);
      tp ^= *(const GAS u32*)((const char*)vc + tile + lane * 4096 + 64);
      tp ^= *(const GAS u32*)((const char*)wo + (size_t)kvh * 524288 + lane * 4096 + sp * 256);
      tp ^= *(const GAS u32*)((const char*)wo + (size_t)kvh * 524288 + 262144 + lane * 4096 + sp * 256);
      u32 o = WS_PA + grp_off * 8192 + (lane & 1) * 4096;
      if (lane >= 2) o = WS_PB + b * 32768 + (lane & 7) * 4096;
      if (lane >= 10) o = WS_TB + ((lane - 10) % 12) * 4096;
      if (lane >= 22) o = WS_ML + grp_off * 256;
      if (lane >= 23) o = WS_QG + grp_off * 16384 + (lane & 3) * 4096;
      tp ^= __hip_atomic_load((GAS u32*)(ws + o), RLX_AGENT);
      tp ^= __hip_atomic_load((GAS u32*)(out + b * DM), RLX_AGENT);
    }
    __builtin_amdgcn_s_sleep(TSLEEP);
    asm volatile("" ::: "memory");
    u32x2 r1[7], rb[7], r2[7];
#pragma unroll
    for (int c = 0; c < 7; ++c) {
      r1[c] = ((const u32x2*)w1)[c * 64 + lane];
      rb[c] = ((const u32x2*)b1)[c * 64 + lane];
      r2[c] = ((const u32x2*)w2)[c * 64 + lane];
    }
    const float b2v = bflo(b2[0]);
    const int idx0 = wg * 24 + tw * 8;
    f32x2 f1[14], fb[14], f2[14];
#pragma unroll
    for (int c = 0; c < 14; ++c) {
      f1[c] = f32x2{bflo(r1[c >> 1][c & 1]), bfhi(r1[c >> 1][c & 1])};
      fb[c] = f32x2{bflo(rb[c >> 1][c & 1]), bfhi(rb[c >> 1][c & 1])};
      f2[c] = f32x2{bflo(r2[c >> 1][c & 1]), bfhi(r2[c >> 1][c & 1])};
    }
    __builtin_amdgcn_s_setprio(0);
    float res = 0.f;
#pragma unroll 1
    for (int e = 0; e < 8; ++e) {
      const int i0 = idx0 + e;
      const float t0 = __uint_as_float((u32)((i0 >= NT_HALF ? i0 - NT_HALF + 0x8000 : i0) + (E_LO << 7)) << 16);
      const f32x2 tt = {t0, t0};
      f32x2 acc = {0.f, 0.f};
#pragma unroll
      for (int c0_ = 0; c0_ < 14; c0_ += 7) {
        f32x2 u[7], z[7];
#pragma unroll
        for (int c = 0; c < 7; ++c) u[c] = __builtin_elementwise_fma(tt, f1[c0_ + c], fb[c0_ + c]);
        SB();
#pragma unroll
        for (int c = 0; c < 7; ++c) z[c] = u[c] * u[c];
        SB();
#pragma unroll
        for (int c = 0; c < 7; ++c) z[c] = __builtin_elementwise_fma(z[c], f32x2{GK1, GK1}, f32x2{GK0, GK0});
        SB();
#pragma unroll
        for (int c = 0; c < 7; ++c) z[c] = z[c] * u[c];
        SB();
#pragma unroll
        for (int c = 0; c < 7; ++c) z[c] = f32x2{__builtin_amdgcn_exp2f(z[c][0]), __builtin_amdgcn_exp2f(z[c][1])};
        SB();
#pragma unroll
        for (int c = 0; c < 7; ++c) { z[c] = z[c] + f32x2{1.f, 1.f}; u[c] = u[c] * f2[c0_ + c]; }
        SB();
#pragma unroll
        for (int c = 0; c < 7; ++c) z[c] = f32x2{__builtin_amdgcn_rcpf(z[c][0]), __builtin_amdgcn_rcpf(z[c][1])};
        SB();
#pragma unroll
        for (int c = 0; c < 7; ++c) acc = __builtin_elementwise_fma(u[c], z[c], acc);
        SB();
      }
      const float a0 = wave_sum(acc[0] + acc[1]);
      if (lane == e) res = a0;
    }
    __builtin_amdgcn_s_setprio(3);
    if (lane < 8) st_gran(ws, WS_TB + (idx0 + lane) * 8, tag, __float_as_uint(rbf(rbf(res) + b2v)));
    asm volatile("" ::"v"(pf));
    asm volatile("" ::"v"(tp));
    STAMPW();
    STAMP(1);
    if (final_wg) {
      lds_wait_ge<8>(ctl + C_ML, NSW);
      const u32 base = (u32)tw * 1024 + lane;
#pragma unroll 1
      for (int half = 0; half < 2; ++half) {
        for (u32 spins = 0;; ++spins) {
          u32x4 g[8];
          bool ok = true;
#pragma unroll
          for (int k = 0; k < 8; ++k) g[k] = ld_gran2(rs, WS_TB + (base + (half * 8 + k) * 64) * 16);
#pragma unroll
          for (int k = 0; k < 8; ++k) {
            ok &= g[k][1] == tag && g[k][3] == tag;
            *(LAS u32x2*)(lds + L_TAB + (base + (half * 8 + k) * 64) * 8) = u32x2{g[k][0], g[k][2]};
          }
          if (__all(ok) || spins > SPIN_CAP) break;
          __builtin_amdgcn_s_sleep(8);
        }
      }
      wait_lgkm();
    }
    STAMP(2);
  }
}

extern "C" void kernel_launch(void* const* d_in, const int* in_sizes, int n_in, void* d_out, int out_size, void* d_ws, size_t ws_size,
                              hipStream_t stream) {
  fused_kernel<<<dim3(NWG), dim3(NTHR), 0, stream>>>((const u16*)d_in[0], (const u16*)d_in[1], (const u16*)d_in[2], (const u16*)d_in[3],
                                                     (const u16*)d_in[4], (const u16*)d_in[6], (const u16*)d_in[7], (const u16*)d_in[8],
                                                     (const u16*)d_in[9], (u16*)d_out, (unsigned char*)d_ws);
}
```

```cpp
#define MEAS 11
#define MEASB 3
#include <hip/hip_runtime.h>
#include <stdint.h>
#include <utility>
#include <type_traits>

typedef unsigned short u16;
typedef unsigned u32;
typedef unsigned long long u64;
typedef u32 u32x2 __attribute__((ext_vector_type(2)));
typedef u32 u32x4 __attribute__((ext_vector_type(4)));
typedef float f32x2 __attribute__((ext_vector_type(2)));
typedef float f32x4 __attribute__((ext_vector_type(4)));
typedef short s16x4 __attribute__((ext_vector_type(4)));
typedef __bf16 bf16x8 __attribute__((ext_vector_type(8)));
#define LAS __attribute__((address_space(3)))
#define GAS __attribute__((address_space(1)))
#define CAS __attribute__((address_space(4)))
#define RLX_AGENT __ATOMIC_RELAXED, __HIP_MEMORY_SCOPE_AGENT

#define K_NT 1
#define V_NT 1
#define W_NT 0
#define WIN 6
#define QTRIG 14
#define MLTRIG 62
#define TSLEEP 100
#define CODE_PF 1
#define CODE_BYTES 17500
#define CRATE 3
#define ASLEEP 2

constexpr int DM = 512, HKV = 8, SK = 4096, FF = 1792;
constexpr int NWG = 256, NSW = 8, NTHR = 768, PARK = 32, NLD = 80;
constexpr int NE = 24, E_LO = 132 - NE, NT_HALF = NE * 128, NTAB = 2 * NT_HALF;
static_assert(NTAB == NWG * 24, "24 table entries per workgroup");
constexpr float SCALE = 0.08838834764831845f, LOG2E = 1.4426950408889634f;
constexpr float GK0 = -2.302208198f, GK1 = GK0 * 0.044715f;
constexpr u32 SPIN_CAP = 1u << 15, LSPIN_CAP = 1u << 19;

constexpr u32 WS_EPOCH = 0, WS_QG = 4096, WS_ML = WS_QG + 1048576, WS_PA = WS_ML + 16384, WS_PB = WS_PA + 524288, WS_TB = WS_PB + 262144,
              WS_END = WS_TB + NTAB * 8, WS_DBG = 2u << 20;
constexpr u32 L_QF = 0, L_CTL = 1024, L_MX = 1088, L_LW = 1216, L_FS = 1344, L_RED2 = 2048, L_HROW = L_RED2 + 4096, L_RED = L_HROW + 2048,
              L_TAB = L_RED + 16384, L_WQ = L_TAB + NTAB * 4, L_END = L_WQ + 16384;
enum { C_Q = 0, C_ML = 1, C_QTRIG = 2, C_MLTRIG = 3, C_TAG = 4 };

__device__ __forceinline__ float bflo(u32 w) { return __builtin_bit_cast(float, w << 16); }
__device__ __forceinline__ float bfhi(u32 w) { return __builtin_bit_cast(float, w & 0xffff0000u); }
__device__ __forceinline__ u32 cvtpk(float lo, float hi) {
  typedef __bf16 bf16x2 __attribute__((ext_vector_type(2)));
  bf16x2 r = {(__bf16)lo, (__bf16)hi};
  return __builtin_bit_cast(u32, r);
}
__device__ __forceinline__ float rbf(float f) { return bflo(cvtpk(f, f)); }
template <int CTRL> __device__ __forceinline__ float dppf(float x) {
  return __builtin_bit_cast(float, __builtin_amdgcn_mov_dpp(__builtin_bit_cast(int, x), CTRL, 0xf, 0xf, true));
}
template <int CTRL> __device__ __forceinline__ u32 dppu(u32 x) { return (u32)__builtin_amdgcn_mov_dpp((int)x, CTRL, 0xf, 0xf, true); }
constexpr int XOR1 = 0xB1, XOR2 = 0x4E, HMIR = 0x141, MIR = 0x140, ROR8 = 0x128;
__device__ __forceinline__ float xrow_sum(float x) {
  auto s = __builtin_amdgcn_permlane16_swap(__float_as_uint(x), __float_as_uint(x), false, false);
  x = __uint_as_float(s[0]) + __uint_as_float(s[1]);
  auto t = __builtin_amdgcn_permlane32_swap(__float_as_uint(x), __float_as_uint(x), false, false);
  return __uint_as_float(t[0]) + __uint_as_float(t[1]);
}
__device__ __forceinline__ float xrow_max(float x) {
  auto s = __builtin_amdgcn_permlane16_swap(__float_as_uint(x), __float_as_uint(x), false, false);
  x = fmaxf(__uint_as_float(s[0]), __uint_as_float(s[1]));
  auto t = __builtin_amdgcn_permlane32_swap(__float_as_uint(x), __float_as_uint(x), false, false);
  return fmaxf(__uint_as_float(t[0]), __uint_as_float(t[1]));
}
__device__ __forceinline__ float wave_sum(float x) {
  x += dppf<XOR1>(x); x += dppf<XOR2>(x); x += dppf<HMIR>(x); x += dppf<MIR>(x);
  return xrow_sum(x);
}
__device__ __forceinline__ f32x4 mfma32(u32x4 a, u32x4 b, f32x4 c) {
  return __builtin_amdgcn_mfma_f32_16x16x32_bf16(__builtin_bit_cast(bf16x8, a), __builtin_bit_cast(bf16x8, b), c, 0, 0, 0);
}
__device__ __forceinline__ f32x4 mfma16(u32x2 a, u32x2 b, f32x4 c) {
  return __builtin_amdgcn_mfma_f32_16x16x16bf16_1k(__builtin_bit_cast(s16x4, a), __builtin_bit_cast(s16x4, b), c, 0, 0, 0);
}
template <int N> __device__ __forceinline__ void wait_vm() { asm volatile("s_waitcnt vmcnt(%0)" ::"n"(N) : "memory"); }
__device__ __forceinline__ void wait_lgkm() { asm volatile("s_waitcnt lgkmcnt(0)" ::: "memory"); }
__device__ __forceinline__ void bar() { asm volatile("s_waitcnt lgkmcnt(0)\n\ts_barrier" ::: "memory"); }
#define SB() __builtin_amdgcn_sched_barrier(0)
#define IC(n) std::integral_constant<int, (n)>{}
template <int L, typename F, int... I> __device__ __forceinline__ void for_impl(F&& f, std::integer_sequence<int, I...>) { (f(std::integral_constant<int, L + I>{}), ...); }
template <int L, int H, typename F> __device__ __forceinline__ void for_range(F&& f) { for_impl<L>(f, std::make_integer_sequence<int, (H > L ? H - L : 0)>{}); }
template <int NT> __device__ __forceinline__ u32x4 ldp(const u32x4* p) { if constexpr (NT) return __builtin_nontemporal_load(p); else return *p; }
__device__ __forceinline__ void st_gran(unsigned char* ws, u32 off, u32 tag, u32 val) {
  __hip_atomic_store((GAS u64*)(ws + off), ((u64)tag << 32) | val, RLX_AGENT);
}
__device__ __forceinline__ u64 ld_gran(unsigned char* ws, u32 off) { return __hip_atomic_load((GAS u64*)(ws + off), RLX_AGENT); }
__device__ __forceinline__ u32x4 ld_gran2(__amdgpu_buffer_rsrc_t rs, u32 off) { return __builtin_amdgcn_raw_buffer_load_b128(rs, off, 0, 16); }
template <int SL> __device__ __forceinline__ void lds_wait_ge(volatile LAS u32* w, u32 n) {
  for (u32 spins = 0; *w < n && spins < LSPIN_CAP; ++spins) __builtin_amdgcn_s_sleep(SL);
  asm volatile("" ::: "memory");
}

#define STAMPR(i) do { if (MEAS == 11 + (i)) t_meas = __builtin_amdgcn_s_memrealtime(); } while (0)
#define STAMPW() do {} while (0)
#define STAMP(i) do { if (MEAS == (i)) t_meas = __builtin_amdgcn_s_memrealtime(); } while (0)

__device__ __forceinline__ float gelu_term(float t, float w1, float b1, float w2, float acc) {
  const float u = fmaf(t, w1, b1);
  const float z = u * fmaf(u * u, GK1, GK0);
  const float e = __builtin_amdgcn_exp2f(z);
  return fmaf(u * w2, __builtin_amdgcn_rcpf(1.0f + e), acc);
}
constexpr int cons_target(int n) { return (n - WIN + 1) < (4 + CRATE * (n - PARK)) ? (n - WIN + 1) : (4 + CRATE * (n - PARK)); }

extern "C" __global__ void __launch_bounds__(NTHR) fused_kernel(const u16* x, const u16* kc, const u16* vc, const u16* wq, const u16* wo,
                                                                const u16* w1, const u16* b1, const u16* w2, const u16* b2, u16* out,
                                                                unsigned char* ws) {
  __shared__ __attribute__((aligned(16))) unsigned char lds[L_END];
  const int lane = threadIdx.x & 63, hwave = __builtin_amdgcn_readfirstlane(threadIdx.x >> 6);
  const int wg = blockIdx.x, kvh = wg & 7, sp = (wg >> 3) & 3, b = wg >> 5;
  const bool final_wg = (kvh == b) && (sp == 0);
  const int li = lane & 15, lg = lane >> 4;
  volatile LAS u32* ctl = (volatile LAS u32*)(lds + L_CTL);
  const __amdgpu_buffer_rsrc_t rs = __builtin_amdgcn_make_buffer_rsrc(ws, 0, WS_END, 0x00020000);
  const u32 grp_off = (u32)(b * HKV + kvh);
  const u64 t_entry = __builtin_amdgcn_s_memrealtime();
  u64 t_meas = t_entry;

  if (hwave == 0) {
    __builtin_amdgcn_s_setprio(3);
    if (lane < 4) ctl[lane] = 0;
    bar();
    SB();
    const int cb = (wg >> 3) & 7, kq = wg >> 6;
    u32x4 xa[8], wt[16];
    const u32x4* xp = (const u32x4*)x + (li & 7) * 64 + (li >> 3) * 4 + lg;
    const u32x4* wp = (const u32x4*)(wq + (size_t)(128 * kq) * 4096 + kvh * 512 + 64 * cb) + (lane >> 3) * 512 + (lane & 7);
#pragma unroll
    for (int k = 0; k < 8; ++k) xa[k] = xp[((k + 2 * kq) & 7) * 8];
#pragma unroll
    for (int k = 0; k < 16; ++k) wt[k] = ldp<W_NT>(wp + k * 8 * 512);
    const u32 tag = *(const CAS u32*)(ws + WS_EPOCH) + 1u;
    SB();
    STAMPR(0);
    {
      float sm = 0.f;
#pragma unroll
      for (int k = 0; k < 8; ++k)
#pragma unroll
        for (int e = 0; e < 4; ++e) sm += bflo(xa[k][e]) + bfhi(xa[k][e]);
      sm += dppf<ROR8>(sm);
      const float mean = xrow_sum(sm) * (1.0f / DM);
      float sq = 0.f;
#pragma unroll
      for (int k = 0; k < 8; ++k)
#pragma unroll
        for (int e = 0; e < 4; ++e) { const float d0 = bflo(xa[k][e]) - mean, d1 = bfhi(xa[k][e]) - mean; sq = fmaf(d0, d0, sq); sq = fmaf(d1, d1, sq); }
      sq += dppf<ROR8>(sq);
      const float var = rbf(xrow_sum(sq) * (1.0f / DM)), mb = rbf(mean);
      const float inv = __builtin_amdgcn_rcpf(rbf(__builtin_amdgcn_sqrtf(rbf(var + 1e-5f))));
#pragma unroll
      for (int k = 0; k < 2; ++k)
#pragma unroll
        for (int e = 0; e < 4; ++e) {
          const u32 d = cvtpk(bflo(xa[k][e]) - mb, bfhi(xa[k][e]) - mb);
          xa[k][e] = cvtpk(bflo(d) * inv, bfhi(d) * inv);
        }
    }
    SB();
    STAMPR(1);
#pragma unroll
    for (int k = 0; k < 16; ++k) *(LAS u32x4*)(lds + L_WQ + k * 1024 + lane * 16) = wt[k];
    wait_lgkm();
    STAMPR(2);
    const u32 troff = L_WQ + (8 * lg + (li >> 2)) * 128 + (li & 3) * 8;
    const bool odd = lane & 1, up = lg & 2;
    const u32 qoff = WS_QG + ((4 * (lg & 1) + (lg & 2) + (lane & 1)) * HKV + kvh) * 16384 + kq * 4096 + (64 * cb + (li & 14)) * 8;
#pragma unroll
    for (int ct = 0; ct < 4; ++ct) {
      f32x4 d0 = {0.f, 0.f, 0.f, 0.f}, d1 = {0.f, 0.f, 0.f, 0.f};
#pragma unroll
      for (int k = 0; k < 4; ++k) {
        const s16x4 b0 = __builtin_amdgcn_ds_read_tr16_b64_v4i16((LAS s16x4*)(lds + troff + k * 4096 + ct * 32));
        const s16x4 b1_ = __builtin_amdgcn_ds_read_tr16_b64_v4i16((LAS s16x4*)(lds + troff + k * 4096 + ct * 32 + 512));
        const u32x2 p0 = __builtin_bit_cast(u32x2, b0), p1 = __builtin_bit_cast(u32x2, b1_);
        if (k & 1) d1 = mfma32(xa[k >> 1], u32x4{p0[0], p0[1], p1[0], p1[1]}, d1);
        else d0 = mfma32(xa[k >> 1], u32x4{p0[0], p0[1], p1[0], p1[1]}, d0);
      }
      const auto s0 = __builtin_amdgcn_permlane32_swap(__float_as_uint(d1[0]), __float_as_uint(d0[2]), false, false);
      const auto s1 = __builtin_amdgcn_permlane32_swap(__float_as_uint(d1[1]), __float_as_uint(d0[3]), false, false);
      const float e0 = up ? d1[2] + __uint_as_float(s0[0]) : d0[0] + __uint_as_float(s0[1]);
      const float e1 = up ? d1[3] + __uint_as_float(s1[0]) : d0[1] + __uint_as_float(s1[1]);
      const float mine = odd ? e1 : e0, got = dppf<XOR1>(odd ? e0 : e1);
      const u32x4 g = {__float_as_uint(odd ? got : mine), tag, __float_as_uint(odd ? mine : got), tag};
      __builtin_amdgcn_raw_buffer_store_b128(g, rs, qoff + ct * 128, 0, 16);
    }
    STAMPR(3);
    STAMPW();
    lds_wait_ge<2>(ctl + C_QTRIG, 1);
    STAMP(1);
    {
      const u32 off = WS_QG + grp_off * 16384 + lane * 16;
      for (u32 spins = 0;; ++spins) {
        u32x4 g[16];
#pragma unroll
        for (int k = 0; k < 16; ++k) g[k] = ld_gran2(rs, off + k * 1024);
        bool ok = true;
#pragma unroll
        for (int k = 0; k < 16; ++k) ok &= g[k][1] == tag && g[k][3] == tag;
#pragma unroll
        for (int h = 0; h < 4; ++h) {
          const float q0 = (__uint_as_float(g[h][0]) + __uint_as_float(g[4 + h][0])) + (__uint_as_float(g[8 + h][0]) + __uint_as_float(g[12 + h][0]));
          const float q1 = (__uint_as_float(g[h][2]) + __uint_as_float(g[4 + h][2])) + (__uint_as_float(g[8 + h][2]) + __uint_as_float(g[12 + h][2]));
          *(LAS u32*)(lds + L_QF + (h * 64 + lane) * 4) = cvtpk(q0, q1);
        }
        if (__all(ok) || spins > SPIN_CAP) break;
        __builtin_amdgcn_s_sleep(8);
      }
      if (lane == 0) ctl[C_TAG] = tag;
      wait_lgkm();
      if (lane == 0) ctl[C_Q] = 1;
    }
    STAMP(2);
    lds_wait_ge<4>(ctl + C_ML, NSW);
    {
      const float mw = *(const LAS float*)(lds + L_MX + (lane & 31) * 4), lw = *(const LAS float*)(lds + L_LW + (lane & 31) * 4);
      float M = fmaxf(mw, dppf<XOR1>(mw)); M = fmaxf(M, dppf<XOR2>(M)); M = fmaxf(M, dppf<HMIR>(M));
      float l = lw * __builtin_amdgcn_exp2f((mw - M) * LOG2E);
      l += dppf<XOR1>(l); l += dppf<XOR2>(l); l += dppf<HMIR>(l);
      const u32 goff = WS_ML + grp_off * 256;
      if (lane < 32 && (lane & 7) < 2) st_gran(ws, goff + sp * 64 + ((lane >> 3) * 2 + (lane & 1)) * 8, tag, __float_as_uint((lane & 1) ? l : M));
      STAMP(3);
      lds_wait_ge<4>(ctl + C_MLTRIG, 1);
      u32x4 g;
      for (u32 spins = 0;; ++spins) {
        g = ld_gran2(rs, goff + (lane & 3) * 64 + ((lane >> 3) & 3) * 16);
        if (__all(g[1] == tag && g[3] == tag) || spins > SPIN_CAP) break;
        __builtin_amdgcn_s_sleep(8);
      }
      const float Mp = __uint_as_float(g[0]);
      float Ms = fmaxf(Mp, dppf<XOR1>(Mp)); Ms = fmaxf(Ms, dppf<XOR2>(Ms));
      float L = __uint_as_float(g[2]) * __builtin_amdgcn_exp2f((Mp - Ms) * LOG2E);
      L += dppf<XOR1>(L); L += dppf<XOR2>(L);
      if (lane < 32) *(LAS float*)(lds + L_FS + lane * 4) = __builtin_amdgcn_exp2f((mw - Ms) * LOG2E) / L;
    }
    STAMP(4);
    bar();
    return;
  }

  if (hwave <= NSW) {
    const int wave = hwave - 1, tid = threadIdx.x - 64;
    bar();
    SB();
    const size_t tile = ((size_t)grp_off * SK + sp * 1024) * 16;
    const u32x4* kb = (const u32x4*)kc + tile + wave * 256;
    const u32x4* vb = (const u32x4*)vc + tile + wave * 256;
    const u32x4* wb = (const u32x4*)wo + (size_t)(kvh * 512 + 64 * wave) * 64 + 16 * sp;
    const u32 klane = ((lane >> 2) & 3) * 16 + 4 * lg + (lane & 3);
    const u32 wlane = lg * 8 * 64 + li;
    u32x4 K[32], V[32], WO[16];
    auto issue = [&](auto nc) {
      constexpr int n = decltype(nc)::value;
      if constexpr (n < 32) {
        K[n] = ldp<K_NT>(kb + (n >> 2) * 2048 + (n & 3) * 64 + klane);
      } else {
        constexpr int m = n - 32, grp = m / 3, r = m % 3;
        if constexpr (r < 2) { constexpr int v = grp * 2 + r; V[v] = ldp<V_NT>(vb + (v >> 2) * 2048 + (v & 3) * 64 + lane); }
        else { WO[grp] = ldp<W_NT>(wb + ((grp >> 3) * 32 + (grp & 7)) * 64 + wlane); }
      }
      if constexpr (n == QTRIG) ctl[C_QTRIG] = 1;
      if constexpr (n == MLTRIG) ctl[C_MLTRIG] = 1;
    };
    for_range<0, WIN>([&](auto n) { issue(n); });
    SB();
    STAMPR(0);
    for_range<WIN, PARK>([&](auto n) {
      wait_vm<WIN - 1>(); SB();
      if constexpr (decltype(n)::value == WIN) STAMPR(1);
      if constexpr (decltype(n)::value == WIN + 6) STAMPR(2);
      if constexpr (decltype(n)::value == WIN + 12) STAMPR(3);
      issue(n); SB();
    });
    STAMPW();
    STAMP(1);
    lds_wait_ge<1>(ctl + C_Q, 1);
    const u32 tag = __builtin_amdgcn_readfirstlane(ctl[C_TAG]);
    const u32x4 qf = *(const LAS u32x4*)(lds + L_QF + (li >> 2) * 256 + lg * 64 + (li & 3) * 16);
    STAMP(2);
    const bool c0 = lane & 1, c1 = lane & 2;
    float S[8];
    u32 pp[8];
    f32x4 acc0 = {0.f, 0.f, 0.f, 0.f}, acc1 = {0.f, 0.f, 0.f, 0.f};
    const u32 half = c0 ? 0xffff0000u : 0x0000ffffu;
    const u32 mask0 = c1 ? 0u : half, mask1 = c1 ? half : 0u;
    u32x4 WB[2][8];
    auto consume_kgroup = [&](auto uc) {
      constexpr int u = decltype(uc)::value;
      const f32x4 z = {0.f, 0.f, 0.f, 0.f};
      float v[4];
#pragma unroll
      for (int t = 0; t < 4; ++t) {
        const f32x4 d = mfma32(K[4 * u + t], qf, z);
        const float lo = c0 ? d[1] : d[0], hi = c0 ? d[3] : d[2];
        v[t] = c1 ? hi : lo;
      }
      const float w0 = (c0 ? v[1] : v[0]) + dppf<XOR1>(c0 ? v[0] : v[1]);
      const float w1_ = (c0 ? v[3] : v[2]) + dppf<XOR1>(c0 ? v[2] : v[3]);
      S[u] = rbf(rbf((c1 ? w1_ : w0) + dppf<XOR2>(c1 ? w0 : w1_)) * SCALE);
    };
    auto softmax = [&]() {
      float m = S[0];
#pragma unroll
      for (int u = 1; u < 8; ++u) m = fmaxf(m, S[u]);
      m = fmaxf(m, dppf<XOR1>(m)); m = fmaxf(m, dppf<XOR2>(m)); m = xrow_max(m);
      const float mc = m * LOG2E;
      float l = 0.f;
#pragma unroll
      for (int u = 0; u < 8; ++u) {
        const float p = __builtin_amdgcn_exp2f(fmaf(S[u], LOG2E, -mc));
        pp[u] = cvtpk(p, p);
        l += bflo(pp[u]);
      }
      l += dppf<XOR1>(l); l += dppf<XOR2>(l); l = xrow_sum(l);
      if ((lane & 3) == 0 && lg == 0) {
        *(LAS float*)(lds + L_MX + ((li >> 2) * 8 + wave) * 4) = m;
        *(LAS float*)(lds + L_LW + ((li >> 2) * 8 + wave) * 4) = l;
      }
      wait_lgkm();
      if (lane == 0) __hip_atomic_fetch_add((LAS u32*)(lds + L_CTL + 4 * C_ML), 1u, __ATOMIC_RELAXED, __HIP_MEMORY_SCOPE_WORKGROUP);
    };
    auto consume_v = [&](auto vc_) {
      constexpr int v = decltype(vc_)::value, u = v >> 2, t = v & 3;
      const u32 pb = dppu<t * 0x55>(pp[u]);
      const u32x2 a = {pb & mask0, pb & mask1};
      acc0 = mfma16(a, u32x2{V[v][0], V[v][1]}, acc0);
      acc1 = mfma16(a, u32x2{V[v][2], V[v][3]}, acc1);
    };
    auto perm_wo = [&](auto ksc) {
      constexpr int ks = decltype(ksc)::value;
#pragma unroll
      for (int c = 0; c < 8; ++c)
#pragma unroll
        for (int d = 0; d < 4; ++d)
          WB[ks][c][d] = __builtin_amdgcn_perm(WO[ks * 8 + 2 * d + 1][c >> 1], WO[ks * 8 + 2 * d][c >> 1], (c & 1) ? 0x07060302u : 0x05040100u);
    };
    auto consume = [&](auto ic) {
      constexpr int i = decltype(ic)::value;
      if constexpr (i < 32) {
        if constexpr ((i & 3) == 3) consume_kgroup(IC(i >> 2));
        if constexpr (i == 31) { STAMP(3); softmax(); STAMP(4); }
      } else {
        constexpr int m = i - 32, grp = m / 3, r = m % 3;
        if constexpr (r < 2) consume_v(IC(grp * 2 + r));
        else if constexpr (grp == 7) perm_wo(IC(0));
      }
    };
    for_range<PARK, NLD>([&](auto nc) {
      constexpr int n = decltype(nc)::value;
      for_range<(n == PARK ? 0 : cons_target(n - 1)), cons_target(n)>([&](auto i) { consume(i); });
      SB(); wait_vm<WIN - 1>(); SB(); issue(nc); SB();
    });
    for_range<cons_target(NLD - 1), NLD>([&](auto i) { consume(i); SB(); });
    STAMP(5);
    *(LAS f32x4*)(lds + L_RED + wave * 2048 + lane * 32) = acc0;
    *(LAS f32x4*)(lds + L_RED + wave * 2048 + lane * 32 + 16) = acc1;
    bar();
    if (wave < 4) {
      const f32x4 fa = *(const LAS f32x4*)(lds + L_FS + wave * 32), fb = *(const LAS f32x4*)(lds + L_FS + wave * 32 + 16);
      f32x2 s = *(const LAS f32x2*)(lds + L_RED + tid * 8) * fa[0];
#pragma unroll
      for (int w = 1; w < 8; ++w) s += *(const LAS f32x2*)(lds + L_RED + w * 2048 + tid * 8) * (w < 4 ? fa[w & 3] : fb[w & 3]);
      st_gran(ws, WS_PA + (grp_off * 4 + sp) * 2048 + tid * 8, tag, cvtpk(s[0], s[1]));
    }
    SB();
    perm_wo(IC(1));
    SB();
    STAMP(6);
    u32x4 A[2];
    {
      const u32 off = WS_PA + (grp_off * 4 + (li & 3)) * 2048 + (32 * wave + 4 * lg) * 8 + (li >> 3) * 128 + ((li >> 2) & 1) * 16;
      u32x4 g;
      bool ok = false;
      for (u32 spins = 0;; ++spins) {
        if (!ok) {
          g = ld_gran2(rs, off);
          ok = g[1] == tag && g[3] == tag;
        }
        if (__all(ok) || spins > SPIN_CAP) break;
        __builtin_amdgcn_s_sleep(ASLEEP);
      }
      A[0] = u32x4{g[0], g[2], dppu<0x104>(g[0]), dppu<0x104>(g[2])};
      A[1] = u32x4{dppu<0x108>(g[0]), dppu<0x108>(g[2]), dppu<0x10C>(g[0]), dppu<0x10C>(g[2])};
    }
    STAMP(7);
    {
      float o[8];
#pragma unroll
      for (int c = 0; c < 8; ++c) {
        f32x4 d = {0.f, 0.f, 0.f, 0.f};
        d = mfma32(A[0], WB[0][c], d);
        d = mfma32(A[1], WB[1][c], d);
        o[c] = (d[0] + d[1]) + (d[2] + d[3]);
      }
      if (lg == 0) {
        *(LAS f32x4*)(lds + L_RED2 + wave * 512 + li * 32) = f32x4{o[0], o[1], o[2], o[3]};
        *(LAS f32x4*)(lds + L_RED2 + wave * 512 + li * 32 + 16) = f32x4{o[4], o[5], o[6], o[7]};
      }
    }
    bar();
    if (wave < 2) {
      float s = *(const LAS float*)(lds + L_RED2 + tid * 4);
#pragma unroll
      for (int w = 1; w < 8; ++w) s += *(const LAS float*)(lds + L_RED2 + w * 512 + tid * 4);
      st_gran(ws, WS_PB + grp_off * 4096 + (128 * sp + tid) * 8, tag, __float_as_uint(s));
    }
    STAMP(8);
    if (!final_wg) return;
    const float xv = bflo(x[b * DM + tid]);
    float a;
    for (u32 spins = 0;; ++spins) {
      u64 g[8];
#pragma unroll
      for (int k = 0; k < 8; ++k) g[k] = ld_gran(ws, WS_PB + (b * HKV + k) * 4096 + tid * 8);
      bool ok = true;
      a = 0.f;
#pragma unroll
      for (int k = 0; k < 8; ++k) { ok &= (u32)(g[k] >> 32) == tag; a += __uint_as_float((u32)g[k]); }
      if (__all(ok) || spins > SPIN_CAP) break;
      __builtin_amdgcn_s_sleep(1);
    }
    STAMP(9);
    const float hv = rbf(xv + rbf(a));
    *(LAS float*)(lds + L_HROW + tid * 4) = hv;
    bar();
    float tv;
    {
      const f32x4 h0 = *(const LAS f32x4*)(lds + L_HROW + lane * 32), h1 = *(const LAS f32x4*)(lds + L_HROW + lane * 32 + 16);
      const float piv = *(const LAS float*)(lds + L_HROW);
      float v[8];
#pragma unroll
      for (int e = 0; e < 4; ++e) { v[e] = h0[e]; v[4 + e] = h1[e]; }
      float s1 = 0.f, q1 = 0.f;
#pragma unroll
      for (int e = 0; e < 8; ++e) { const float d = v[e] - piv; s1 += d; q1 = fmaf(d, d, q1); }
      s1 = wave_sum(s1) * (1.0f / DM); q1 = wave_sum(q1) * (1.0f / DM);
      const float mb1 = rbf(piv + s1);
      const float inv1 = __builtin_amdgcn_rcpf(rbf(__builtin_amdgcn_sqrtf(rbf(rbf(fmaxf(fmaf(-s1, s1, q1), 0.f)) + 1e-5f))));
      float s2 = 0.f, q2 = 0.f;
#pragma unroll
      for (int e = 0; e < 8; e += 2) {
        const u32 d = cvtpk(v[e] - mb1, v[e + 1] - mb1);
        const u32 n = cvtpk(bflo(d) * inv1, bfhi(d) * inv1);
        const float n0 = bflo(n), n1 = bfhi(n);
        s2 += n0 + n1; q2 = fmaf(n0, n0, q2); q2 = fmaf(n1, n1, q2);
      }
      const float t1 = rbf(rbf(hv - mb1) * inv1);
      s2 = wave_sum(s2) * (1.0f / DM); q2 = wave_sum(q2) * (1.0f / DM);
      const float inv2 = __builtin_amdgcn_rcpf(rbf(__builtin_amdgcn_sqrtf(rbf(rbf(fmaxf(fmaf(-s2, s2, q2), 0.f)) + 1e-5f))));
      tv = rbf(rbf(t1 - rbf(s2)) * inv2);
    }
    const u32 bits = __float_as_uint(tv) >> 16;
    const u32 rel = (bits & 0x7fffu) - (E_LO << 7);
    const bool in = rel < (u32)NT_HALF;
    float y = *(const LAS float*)(lds + L_TAB + ((in ? rel : 0u) + (bits >> 15) * NT_HALF) * 4);
    u64 mm = __ballot(!in);
    while (mm) {
      const int src = __builtin_ctzll(mm);
      mm &= mm - 1;
      const float ts = __builtin_bit_cast(float, __builtin_amdgcn_readlane(__float_as_uint(tv), src));
      float acc = 0.f;
#pragma unroll 1
      for (int f = lane; f < FF; f += 64) acc = gelu_term(ts, bflo(w1[f]), bflo(b1[f]), bflo(w2[f]), acc);
      acc = wave_sum(acc);
      if (lane == src) y = rbf(rbf(acc) + bflo(b2[0]));
    }
    out[b * DM + tid] = (u16)cvtpk(hv + y, 0.f);
    if (b == 0 && tid == 0) __hip_atomic_store((GAS u32*)(ws + WS_EPOCH), tag, RLX_AGENT);
    STAMP(10);
    if (b == MEASB && wave == 2) {
      const u64 t_end = __builtin_amdgcn_s_memrealtime(), d = (t_meas - t_entry) * 4;
      while (__builtin_amdgcn_s_memrealtime() - t_end < d) __builtin_amdgcn_s_sleep(1);
    }
    return;
  }

  {
    const int tw = hwave - NSW - 1;
    __builtin_amdgcn_s_setprio(3);
    bar();
    SB();
    const u32 tag = *(const CAS u32*)(ws + WS_EPOCH) + 1u;
    u32 pf = 0;
    if (tw == 0) {
      u64 pc; u32 back;
      asm volatile("s_getpc_b64 %0\n.Lpf_here:\n\ts_mov_b32 %1, .Lpf_here-fused_kernel" : "=s"(pc), "=s"(back));
      const u32 o = ((wg >> 3) * 8 + lane) * 128;
      if (lane < 8 && o + 4 <= CODE_BYTES) pf = *(const GAS u32*)(pc - back + o);
    }
    __builtin_amdgcn_s_sleep(TSLEEP);
    asm volatile("" ::: "memory");
    u32x2 r1[7], rb[7], r2[7];
#pragma unroll
    for (int c = 0; c < 7; ++c) {
      r1[c] = ((const u32x2*)w1)[c * 64 + lane];
      rb[c] = ((const u32x2*)b1)[c * 64 + lane];
      r2[c] = ((const u32x2*)w2)[c * 64 + lane];
    }
    const float b2v = bflo(b2[0]);
    const int idx0 = wg * 24 + tw * 8;
    f32x2 f1[14], fb[14], f2[14];
#pragma unroll
    for (int c = 0; c < 14; ++c) {
      f1[c] = f32x2{bflo(r1[c >> 1][c & 1]), bfhi(r1[c >> 1][c & 1])};
      fb[c] = f32x2{bflo(rb[c >> 1][c & 1]), bfhi(rb[c >> 1][c & 1])};
      f2[c] = f32x2{bflo(r2[c >> 1][c & 1]), bfhi(r2[c >> 1][c & 1])};
    }
    __builtin_amdgcn_s_setprio(0);
    float res = 0.f;
#pragma unroll 1
    for (int e = 0; e < 8; ++e) {
      const int i0 = idx0 + e;
      const float t0 = __uint_as_float((u32)((i0 >= NT_HALF ? i0 - NT_HALF + 0x8000 : i0) + (E_LO << 7)) << 16);
      const f32x2 tt = {t0, t0};
      f32x2 acc = {0.f, 0.f};
#pragma unroll
      for (int c0_ = 0; c0_ < 14; c0_ += 7) {
        f32x2 u[7], z[7];
#pragma unroll
        for (int c = 0; c < 7; ++c) u[c] = __builtin_elementwise_fma(tt, f1[c0_ + c], fb[c0_ + c]);
        SB();
#pragma unroll
        for (int c = 0; c < 7; ++c) z[c] = u[c] * u[c];
        SB();
#pragma unroll
        for (int c = 0; c < 7; ++c) z[c] = __builtin_elementwise_fma(z[c], f32x2{GK1, GK1}, f32x2{GK0, GK0});
        SB();
#pragma unroll
        for (int c = 0; c < 7; ++c) z[c] = z[c] * u[c];
        SB();
#pragma unroll
        for (int c = 0; c < 7; ++c) z[c] = f32x2{__builtin_amdgcn_exp2f(z[c][0]), __builtin_amdgcn_exp2f(z[c][1])};
        SB();
#pragma unroll
        for (int c = 0; c < 7; ++c) { z[c] = z[c] + f32x2{1.f, 1.f}; u[c] = u[c] * f2[c0_ + c]; }
        SB();
#pragma unroll
        for (int c = 0; c < 7; ++c) z[c] = f32x2{__builtin_amdgcn_rcpf(z[c][0]), __builtin_amdgcn_rcpf(z[c][1])};
        SB();
#pragma unroll
        for (int c = 0; c < 7; ++c) acc = __builtin_elementwise_fma(u[c], z[c], acc);
        SB();
      }
      const float a0 = wave_sum(acc[0] + acc[1]);
      if (lane == e) res = a0;
    }
    __builtin_amdgcn_s_setprio(3);
    if (lane < 8) st_gran(ws, WS_TB + (idx0 + lane) * 8, tag, __float_as_uint(rbf(rbf(res) + b2v)));
    asm volatile("" ::"v"(pf));
    STAMPW();
    STAMP(1);
    if (final_wg) {
      lds_wait_ge<8>(ctl + C_ML, NSW);
      const u32 base = (u32)tw * 1024 + lane;
#pragma unroll 1
      for (int half = 0; half < 2; ++half) {
        for (u32 spins = 0;; ++spins) {
          u32x4 g[8];
          bool ok = true;
#pragma unroll
          for (int k = 0; k < 8; ++k) g[k] = ld_gran2(rs, WS_TB + (base + (half * 8 + k) * 64) * 16);
#pragma unroll
          for (int k = 0; k < 8; ++k) {
            ok &= g[k][1] == tag && g[k][3] == tag;
            *(LAS u32x2*)(lds + L_TAB + (base + (half * 8 + k) * 64) * 8) = u32x2{g[k][0], g[k][2]};
          }
          if (__all(ok) || spins > SPIN_CAP) break;
          __builtin_amdgcn_s_sleep(8);
        }
      }
      wait_lgkm();
    }
    STAMP(2);
  }
}

extern "C" void kernel_launch(void* const* d_in, const int* in_sizes, int n_in, void* d_out, int out_size, void* d_ws, size_t ws_size,
                              hipStream_t stream) {
  fused_kernel<<<dim3(NWG), dim3(NTHR), 0, stream>>>((const u16*)d_in[0], (const u16*)d_in[1], (const u16*)d_in[2], (const u16*)d_in[3],
                                                     (const u16*)d_in[4], (const u16*)d_in[6], (const u16*)d_in[7], (const u16*)d_in[8],
                                                     (const u16*)d_in[9], (u16*)d_out, (unsigned char*)d_ws);
}
```

```cpp
#include <hip/hip_runtime.h>
#include <stdint.h>
#include <utility>
#include <type_traits>

typedef unsigned short u16;
typedef unsigned u32;
typedef unsigned long long u64;
typedef u32 u32x2 __attribute__((ext_vector_type(2)));
typedef u32 u32x4 __attribute__((ext_vector_type(4)));
typedef float f32x2 __attribute__((ext_vector_type(2)));
typedef float f32x4 __attribute__((ext_vector_type(4)));
typedef short s16x4 __attribute__((ext_vector_type(4)));
typedef __bf16 bf16x8 __attribute__((ext_vector_type(8)));
#define LAS __attribute__((address_space(3)))
#define GAS __attribute__((address_space(1)))
#define CAS __attribute__((address_space(4)))
#define RLX_AGENT __ATOMIC_RELAXED, __HIP_MEMORY_SCOPE_AGENT

#define K_NT 1
#define V_NT 1
#define W_NT 1
#define TAB_N 12
#define WOPRE 6
#define WOD 4
#define WO_T0 400
#define WO_DT 40
#define PACE 300
#define POFF 0
#define WCAP 10
#define XV 4
#define LAG 7
#define WO_AT 46
#define WIN0 3
#define WIN2 8
#define KPS 1
#define WIN 5
#define QTRIG 16
#define MLTRIG 54
#define TSLEEP 60
#define CODE_PF 1
#define CODE_BYTES 20000
#define CRATE 3
#define ASLEEP 2

constexpr int DM = 512, HKV = 8, SK = 4096, FF = 1792;
constexpr int NWG = 256, NSW = 8, NTHR = 768, NLD = 72;
constexpr int NE = 24, E_LO = 132 - NE, NT_HALF = NE * 128, NTAB = 2 * NT_HALF;
static_assert(NTAB == NWG * 24, "24 table entries per workgroup");
constexpr float SCALE = 0.08838834764831845f, LOG2E = 1.4426950408889634f;
constexpr float GK0 = -2.302208198f, GK1 = GK0 * 0.044715f;
constexpr u32 SPIN_CAP = 1u << 15, LSPIN_CAP = 1u << 19;

constexpr u32 WS_EPOCH = 0, WS_QG = 4096, WS_ML = WS_QG + 1048576, WS_PA = WS_ML + 16384, WS_PB = WS_PA + 524288, WS_TB = WS_PB + 262144,
              WS_END = WS_TB + NTAB * 4, WS_DBG = 2u << 20;
constexpr u32 L_QF = 0, L_CTL = 1024, L_MX = 1088, L_LW = 1216, L_FS = 1344, L_HROW = 1536, L_OA = 2048, OA_W = 528, L_RED = L_OA + 16 * OA_W,
              L_TAB = L_RED + 16384, L_WQ = L_TAB + NTAB * 2, L_TW = L_WQ + 16384, L_WO = L_TW + 10752, L_END = L_WO + 32768;
enum { C_Q = 0, C_ML = 1, C_QTRIG = 2, C_MLTRIG = 3, C_TAG = 4, C_TW = 5, C_WOTRIG = 6 };

__device__ __forceinline__ float bflo(u32 w) { return __builtin_bit_cast(float, w << 16); }
__device__ __forceinline__ float bfhi(u32 w) { return __builtin_bit_cast(float, w & 0xffff0000u); }
__device__ __forceinline__ u32 cvtpk(float lo, float hi) {
  typedef __bf16 bf16x2 __attribute__((ext_vector_type(2)));
  bf16x2 r = {(__bf16)lo, (__bf16)hi};
  return __builtin_bit_cast(u32, r);
}
__device__ __forceinline__ float rbf(float f) { return bflo(cvtpk(f, f)); }
template <int CTRL> __device__ __forceinline__ float dppf(float x) {
  return __builtin_bit_cast(float, __builtin_amdgcn_mov_dpp(__builtin_bit_cast(int, x), CTRL, 0xf, 0xf, true));
}
template <int CTRL> __device__ __forceinline__ u32 dppu(u32 x) { return (u32)__builtin_amdgcn_mov_dpp((int)x, CTRL, 0xf, 0xf, true); }
constexpr int XOR1 = 0xB1, XOR2 = 0x4E, HMIR = 0x141, MIR = 0x140, ROR8 = 0x128;
__device__ __forceinline__ float xrow_sum(float x) {
  auto s = __builtin_amdgcn_permlane16_swap(__float_as_uint(x), __float_as_uint(x), false, false);
  x = __uint_as_float(s[0]) + __uint_as_float(s[1]);
  auto t = __builtin_amdgcn_permlane32_swap(__float_as_uint(x), __float_as_uint(x), false, false);
  return __uint_as_float(t[0]) + __uint_as_float(t[1]);
}
__device__ __forceinline__ float xrow_max(float x) {
  auto s = __builtin_amdgcn_permlane16_swap(__float_as_uint(x), __float_as_uint(x), false, false);
  x = fmaxf(__uint_as_float(s[0]), __uint_as_float(s[1]));
  auto t = __builtin_amdgcn_permlane32_swap(__float_as_uint(x), __float_as_uint(x), false, false);
  return fmaxf(__uint_as_float(t[0]), __uint_as_float(t[1]));
}
__device__ __forceinline__ float wave_sum(float x) {
  x += dppf<XOR1>(x); x += dppf<XOR2>(x); x += dppf<HMIR>(x); x += dppf<MIR>(x);
  return xrow_sum(x);
}
__device__ __forceinline__ f32x4 mfma32(u32x4 a, u32x4 b, f32x4 c) {
  return __builtin_amdgcn_mfma_f32_16x16x32_bf16(__builtin_bit_cast(bf16x8, a), __builtin_bit_cast(bf16x8, b), c, 0, 0, 0);
}
__device__ __forceinline__ f32x4 mfma16(u32x2 a, u32x2 b, f32x4 c) {
  return __builtin_amdgcn_mfma_f32_16x16x16bf16_1k(__builtin_bit_cast(s16x4, a), __builtin_bit_cast(s16x4, b), c, 0, 0, 0);
}
template <int N> __device__ __forceinline__ void wait_vm() { asm volatile("s_waitcnt vmcnt(%0)" ::"n"(N) : "memory"); }
__device__ __forceinline__ void wait_lgkm() { asm volatile("s_waitcnt lgkmcnt(0)" ::: "memory"); }
__device__ __forceinline__ void bar() { asm volatile("s_waitcnt lgkmcnt(0)\n\ts_barrier" ::: "memory"); }
#define SB() __builtin_amdgcn_sched_barrier(0)
#define IC(n) std::integral_constant<int, (n)>{}
template <int L, typename F, int... I> __device__ __forceinline__ void for_impl(F&& f, std::integer_sequence<int, I...>) { (f(std::integral_constant<int, L + I>{}), ...); }
template <int L, int H, typename F> __device__ __forceinline__ void for_range(F&& f) { for_impl<L>(f, std::make_integer_sequence<int, (H > L ? H - L : 0)>{}); }
template <int NT> __device__ __forceinline__ u32x4 ldp(const u32x4* p) { if constexpr (NT) return __builtin_nontemporal_load(p); else return *p; }
__device__ __forceinline__ void st_gran(unsigned char* ws, u32 off, u32 tag, u32 val) {
  __hip_atomic_store((GAS u64*)(ws + off), ((u64)tag << 32) | val, RLX_AGENT);
}
__device__ __forceinline__ u64 ld_gran(unsigned char* ws, u32 off) { return __hip_atomic_load((GAS u64*)(ws + off), RLX_AGENT); }
__device__ __forceinline__ u32x4 ld_gran2(__amdgpu_buffer_rsrc_t rs, u32 off) { return __builtin_amdgcn_raw_buffer_load_b128(rs, off, 0, 16); }
template <int SL> __device__ __forceinline__ void lds_wait_ge(volatile LAS u32* w, u32 n) {
  for (u32 spins = 0; *w < n && spins < LSPIN_CAP; ++spins) __builtin_amdgcn_s_sleep(SL);
  asm volatile("" ::: "memory");
}

#define STAMPR(i) do {} while (0)
#define STAMPW() do {} while (0)
#define STAMP(i) do {} while (0)

__device__ __forceinline__ float gelu_term(float t, float w1, float b1, float w2, float acc) {
  const float u = fmaf(t, w1, b1);
  const float z = u * fmaf(u * u, GK1, GK0);
  const float e = __builtin_amdgcn_exp2f(z);
  return fmaf(u * w2, __builtin_amdgcn_rcpf(1.0f + e), acc);
}
constexpr int vdone(int n) {
  const int a = 2 * (n - (40 + XV)), c = n - 32 - LAG;
  return a < 0 ? 0 : (a < c ? a : c);
}

extern "C" __global__ void __launch_bounds__(NTHR) fused_kernel(const u16* x, const u16* kc, const u16* vc, const u16* wq, const u16* wo,
                                                                const u16* w1, const u16* b1, const u16* w2, const u16* b2, u16* out,
                                                                unsigned char* ws) {
  __shared__ __attribute__((aligned(16))) unsigned char lds[L_END];
  const int lane = threadIdx.x & 63, hwave = __builtin_amdgcn_readfirstlane(threadIdx.x >> 6);
  const int wg = blockIdx.x, kvh = wg & 7, sp = (wg >> 3) & 3, b = wg >> 5;
  const bool final_wg = (kvh == b) && (sp == 0);
  const int li = lane & 15, lg = lane >> 4;
  volatile LAS u32* ctl = (volatile LAS u32*)(lds + L_CTL);
  const __amdgpu_buffer_rsrc_t rs = __builtin_amdgcn_make_buffer_rsrc(ws, 0, WS_END, 0x00020000);
  const u32 grp_off = (u32)(b * HKV + kvh);
  const int kh = (b >> 1) & 1, ct8 = (b & 1) * 4 + sp;

  if (hwave == 0) {
    __builtin_amdgcn_s_setprio(3);
    if (lane < 8) ctl[lane] = 0;
    const int cb = (wg >> 3) & 7, kq = wg >> 6;
    u32x4 xa[8], wt[16];
    const u32x4* xp = (const u32x4*)x + (li & 7) * 64 + (li >> 3) * 4 + lg;
    const u32x4* wp = (const u32x4*)(wq + (size_t)(128 * kq) * 4096 + kvh * 512 + 64 * cb) + (lane >> 3) * 512 + (lane & 7);
#pragma unroll
    for (int k = 0; k < 8; ++k) xa[k] = xp[((k + 2 * kq) & 7) * 8];
#pragma unroll
    for (int k = 0; k < 16; ++k) wt[k] = ldp<W_NT>(wp + k * 8 * 512);
    SB();
    bar();
    SB();
    const u32 tag = *(const CAS u32*)(ws + WS_EPOCH) + 1u;
    STAMPR(0);
    {
      float sm = 0.f;
#pragma unroll
      for (int k = 0; k < 8; ++k)
#pragma unroll
        for (int e = 0; e < 4; ++e) sm += bflo(xa[k][e]) + bfhi(xa[k][e]);
      sm += dppf<ROR8>(sm);
      const float mean = xrow_sum(sm) * (1.0f / DM);
      float sq = 0.f;
#pragma unroll
      for (int k = 0; k < 8; ++k)
#pragma unroll
        for (int e = 0; e < 4; ++e) { const float d0 = bflo(xa[k][e]) - mean, d1 = bfhi(xa[k][e]) - mean; sq = fmaf(d0, d0, sq); sq = fmaf(d1, d1, sq); }
      sq += dppf<ROR8>(sq);
      const float var = rbf(xrow_sum(sq) * (1.0f / DM)), mb = rbf(mean);
      const float inv = __builtin_amdgcn_rcpf(rbf(__builtin_amdgcn_sqrtf(rbf(var + 1e-5f))));
#pragma unroll
      for (int k = 0; k < 2; ++k)
#pragma unroll
        for (int e = 0; e < 4; ++e) {
          const u32 d = cvtpk(bflo(xa[k][e]) - mb, bfhi(xa[k][e]) - mb);
          xa[k][e] = cvtpk(bflo(d) * inv, bfhi(d) * inv);
        }
    }
    SB();
    STAMPR(1);
#pragma unroll
    for (int k = 0; k < 16; ++k) *(LAS u32x4*)(lds + L_WQ + k * 1024 + lane * 16) = wt[k];
    wait_lgkm();
    STAMPR(2);
    const u32 troff = L_WQ + (8 * lg + (li >> 2)) * 128 + (li & 3) * 8;
    const bool odd = lane & 1, up = lg & 2;
    const u32 qoff = WS_QG + ((4 * (lg & 1) + (lg & 2) + (lane & 1)) * HKV + kvh) * 16384 + kq * 4096 + (64 * cb + (li & 14)) * 8;
#pragma unroll
    for (int ct = 0; ct < 4; ++ct) {
      f32x4 d0 = {0.f, 0.f, 0.f, 0.f}, d1 = {0.f, 0.f, 0.f, 0.f};
#pragma unroll
      for (int k = 0; k < 4; ++k) {
        const s16x4 b0 = __builtin_amdgcn_ds_read_tr16_b64_v4i16((LAS s16x4*)(lds + troff + k * 4096 + ct * 32));
        const s16x4 b1_ = __builtin_amdgcn_ds_read_tr16_b64_v4i16((LAS s16x4*)(lds + troff + k * 4096 + ct * 32 + 512));
        const u32x2 p0 = __builtin_bit_cast(u32x2, b0), p1 = __builtin_bit_cast(u32x2, b1_);
        if (k & 1) d1 = mfma32(xa[k >> 1], u32x4{p0[0], p0[1], p1[0], p1[1]}, d1);
        else d0 = mfma32(xa[k >> 1], u32x4{p0[0], p0[1], p1[0], p1[1]}, d0);
      }
      const auto s0 = __builtin_amdgcn_permlane32_swap(__float_as_uint(d1[0]), __float_as_uint(d0[2]), false, false);
      const auto s1 = __builtin_amdgcn_permlane32_swap(__float_as_uint(d1[1]), __float_as_uint(d0[3]), false, false);
      const float e0 = up ? d1[2] + __uint_as_float(s0[0]) : d0[0] + __uint_as_float(s0[1]);
      const float e1 = up ? d1[3] + __uint_as_float(s1[0]) : d0[1] + __uint_as_float(s1[1]);
      const float mine = odd ? e1 : e0, got = dppf<XOR1>(odd ? e0 : e1);
      const u32x4 g = {__float_as_uint(odd ? got : mine), tag, __float_as_uint(odd ? mine : got), tag};
      __builtin_amdgcn_raw_buffer_store_b128(g, rs, qoff + ct * 128, 0, 16);
    }
    STAMPR(3);
    STAMPW();
    lds_wait_ge<2>(ctl + C_QTRIG, 1);
    STAMP(1);
    {
      const u32 off = WS_QG + grp_off * 16384 + lane * 16;
      for (u32 spins = 0;; ++spins) {
        u32x4 g[16];
#pragma unroll
        for (int k = 0; k < 16; ++k) g[k] = ld_gran2(rs, off + k * 1024);
        bool ok = true;
#pragma unroll
        for (int k = 0; k < 16; ++k) ok &= g[k][1] == tag && g[k][3] == tag;
#pragma unroll
        for (int h = 0; h < 4; ++h) {
          const float q0 = (__uint_as_float(g[h][0]) + __uint_as_float(g[4 + h][0])) + (__uint_as_float(g[8 + h][0]) + __uint_as_float(g[12 + h][0]));
          const float q1 = (__uint_as_float(g[h][2]) + __uint_as_float(g[4 + h][2])) + (__uint_as_float(g[8 + h][2]) + __uint_as_float(g[12 + h][2]));
          *(LAS u32*)(lds + L_QF + (h * 64 + lane) * 4) = cvtpk(q0, q1);
        }
        if (__all(ok) || spins > SPIN_CAP) break;
        __builtin_amdgcn_s_sleep(8);
      }
      if (lane == 0) ctl[C_TAG] = tag;
      wait_lgkm();
      if (lane == 0) ctl[C_Q] = 1;
    }
    STAMP(2);
    lds_wait_ge<4>(ctl + C_ML, NSW);

    {
      const float mw = *(const LAS float*)(lds + L_MX + (lane & 31) * 4), lw = *(const LAS float*)(lds + L_LW + (lane & 31) * 4);
      float M = fmaxf(mw, dppf<XOR1>(mw)); M = fmaxf(M, dppf<XOR2>(M)); M = fmaxf(M, dppf<HMIR>(M));
      float l = lw * __builtin_amdgcn_exp2f((mw - M) * LOG2E);
      l += dppf<XOR1>(l); l += dppf<XOR2>(l); l += dppf<HMIR>(l);
      const u32 goff = WS_ML + grp_off * 256;
      if (lane < 32 && (lane & 7) < 2) st_gran(ws, goff + sp * 64 + ((lane >> 3) * 2 + (lane & 1)) * 8, tag, __float_as_uint((lane & 1) ? l : M));
      STAMP(3);
      lds_wait_ge<4>(ctl + C_MLTRIG, 1);
      u32x4 g;
      for (u32 spins = 0;; ++spins) {
        g = ld_gran2(rs, goff + (lane & 3) * 64 + ((lane >> 3) & 3) * 16);
        if (__all(g[1] == tag && g[3] == tag) || spins > SPIN_CAP) break;
        __builtin_amdgcn_s_sleep(8);
      }
      const float Mp = __uint_as_float(g[0]);
      float Ms = fmaxf(Mp, dppf<XOR1>(Mp)); Ms = fmaxf(Ms, dppf<XOR2>(Ms));
      float L = __uint_as_float(g[2]) * __builtin_amdgcn_exp2f((Mp - Ms) * LOG2E);
      L += dppf<XOR1>(L); L += dppf<XOR2>(L);
      if (lane < 32) *(LAS float*)(lds + L_FS + lane * 4) = __builtin_amdgcn_exp2f((mw - Ms) * LOG2E) / L;
    }
    STAMP(4);
    bar();
    return;
  }

  if (hwave <= NSW) {
    const int wave = hwave - 1, tid = threadIdx.x - 64;
    const u32 t0 = (u32)__builtin_amdgcn_s_memrealtime() + POFF + (u32)(wave * PACE / 80);
    auto pace = [&](auto nc) {
      constexpr int n = decltype(nc)::value;
      wait_vm<WCAP - 1>();
      while ((int)((u32)__builtin_amdgcn_s_memrealtime() - t0) < n * PACE / 10) __builtin_amdgcn_s_sleep(1);
      asm volatile("" ::: "memory");
    };
    const size_t tile = ((size_t)grp_off * SK + sp * 1024) * 16;
    const u32x4* kb = (const u32x4*)kc + tile + wave * 256;
    const u32x4* vb = (const u32x4*)vc + tile + wave * 256;
    const u32 klane = ((lane >> 2) & 3) * 16 + 4 * lg + (lane & 3);
    u32x4 K[32], V[32];
    auto issue = [&](auto nc) {
      constexpr int n = decltype(nc)::value;
      if constexpr (n < 32) K[n] = ldp<K_NT>(kb + (n >> 2) * 2048 + (n & 3) * 64 + klane);
      else V[n - 32] = ldp<V_NT>(vb + ((n - 32) >> 2) * 2048 + (n & 3) * 64 + lane);
    };
    for_range<0, WIN0>([&](auto n) { issue(n); });
    SB();
    bar();
    SB();
    STAMPR(0);
    for_range<WIN0, 32 + XV>([&](auto nc) {
      constexpr int n = decltype(nc)::value;
      pace(nc);
      SB();
      if constexpr (n == 6) STAMPR(1);
      if constexpr (n == 12) STAMPR(2);
      if constexpr (n == 18) STAMPR(3);
      issue(nc);
      if constexpr (n == QTRIG) ctl[C_QTRIG] = 1;
      SB();
    });
    STAMPW();
    STAMP(1);
    lds_wait_ge<1>(ctl + C_Q, 1);
    const u32 tag = __builtin_amdgcn_readfirstlane(ctl[C_TAG]);
    const u32x4 qf = *(const LAS u32x4*)(lds + L_QF + (li >> 2) * 256 + lg * 64 + (li & 3) * 16);
    STAMP(2);
    const bool c0 = lane & 1, c1 = lane & 2;
    float S[8];
    u32 pp[8];
    f32x4 acc0 = {0.f, 0.f, 0.f, 0.f}, acc1 = {0.f, 0.f, 0.f, 0.f};
    const u32 half = c0 ? 0xffff0000u : 0x0000ffffu;
    const u32 mask0 = c1 ? 0u : half, mask1 = c1 ? half : 0u;
    auto consume_kgroup = [&](auto uc) {
      constexpr int u = decltype(uc)::value;
      const f32x4 z = {0.f, 0.f, 0.f, 0.f};
      float v[4];
#pragma unroll
      for (int t = 0; t < 4; ++t) {
        const f32x4 d = mfma32(K[4 * u + t], qf, z);
        const float lo = c0 ? d[1] : d[0], hi = c0 ? d[3] : d[2];
        v[t] = c1 ? hi : lo;
      }
      const float w0 = (c0 ? v[1] : v[0]) + dppf<XOR1>(c0 ? v[0] : v[1]);
      const float w1_ = (c0 ? v[3] : v[2]) + dppf<XOR1>(c0 ? v[2] : v[3]);
      S[u] = rbf(rbf((c1 ? w1_ : w0) + dppf<XOR2>(c1 ? w0 : w1_)) * SCALE);
    };
    auto softmax = [&]() {
      float m = S[0];
#pragma unroll
      for (int u = 1; u < 8; ++u) m = fmaxf(m, S[u]);
      m = fmaxf(m, dppf<XOR1>(m)); m = fmaxf(m, dppf<XOR2>(m)); m = xrow_max(m);
      const float mc = m * LOG2E;
      float l = 0.f;
#pragma unroll
      for (int u = 0; u < 8; ++u) {
        const float p = __builtin_amdgcn_exp2f(fmaf(S[u], LOG2E, -mc));
        pp[u] = cvtpk(p, p);
        l += bflo(pp[u]);
      }
      l += dppf<XOR1>(l); l += dppf<XOR2>(l); l = xrow_sum(l);
      if ((lane & 3) == 0 && lg == 0) {
        *(LAS float*)(lds + L_MX + ((li >> 2) * 8 + wave) * 4) = m;
        *(LAS float*)(lds + L_LW + ((li >> 2) * 8 + wave) * 4) = l;
      }
      wait_lgkm();
      if (lane == 0) __hip_atomic_fetch_add((LAS u32*)(lds + L_CTL + 4 * C_ML), 1u, __ATOMIC_RELAXED, __HIP_MEMORY_SCOPE_WORKGROUP);
    };
    auto consume_v = [&](auto vc_) {
      constexpr int v = decltype(vc_)::value, u = v >> 2, t = v & 3;
      const u32 pb = dppu<t * 0x55>(pp[u]);
      const u32x2 a = {pb & mask0, pb & mask1};
      acc0 = mfma16(a, u32x2{V[v][0], V[v][1]}, acc0);
      acc1 = mfma16(a, u32x2{V[v][2], V[v][3]}, acc1);
    };
    constexpr int N1 = 32 + XV, N2 = N1 + 8;
    for_range<N1, 64>([&](auto nc) {
      constexpr int n = decltype(nc)::value;
      if constexpr (n < N2) consume_kgroup(IC(n - N1));
      if constexpr (n == N2) { STAMP(3); softmax(); STAMP(4); }
      if constexpr (n > N2) for_range<vdone(n - 1), vdone(n)>([&](auto v) { consume_v(v); });
      SB();
      pace(nc);
      SB();
      issue(nc);
      if constexpr (n == MLTRIG) ctl[C_MLTRIG] = 1;
      SB();
    });
    for_range<vdone(63), 32>([&](auto v) { consume_v(v); SB(); });
    STAMP(5);
    *(LAS f32x4*)(lds + L_RED + wave * 2048 + lane * 32) = acc0;
    *(LAS f32x4*)(lds + L_RED + wave * 2048 + lane * 32 + 16) = acc1;
    bar();
    if (wave < 4) {
      const f32x4 fa = *(const LAS f32x4*)(lds + L_FS + wave * 32), fb = *(const LAS f32x4*)(lds + L_FS + wave * 32 + 16);
      f32x2 s = *(const LAS f32x2*)(lds + L_RED + tid * 8) * fa[0];
#pragma unroll
      for (int w = 1; w < 8; ++w) s += *(const LAS f32x2*)(lds + L_RED + w * 2048 + tid * 8) * (w < 4 ? fa[w & 3] : fb[w & 3]);
      st_gran(ws, WS_PA + (grp_off * 4 + sp) * 2048 + tid * 8, tag, cvtpk(s[0], s[1]));
    }
    STAMP(6);
    u32x4 WB[8];
    {
      const u32 off = WS_PA + ((((b & 4) + (wave >> 2)) * HKV + kvh) * 4 + (wave & 3)) * 2048 + kh * 1024 + lane * 16;
      constexpr u32 D8 = 2 * HKV * 4 * 2048;
      u32x4 g0 = ld_gran2(rs, off), g1 = ld_gran2(rs, off + D8);
      SB();
      if (wave >= 4) {
        const u32 troff = L_WO + (8 * lg + (li >> 2)) * 128 + (wave & 3) * 32 + (li & 3) * 8;
#pragma unroll
        for (int k = 0; k < 8; ++k) {
          const u32x2 p0 = __builtin_bit_cast(u32x2, __builtin_amdgcn_ds_read_tr16_b64_v4i16((LAS s16x4*)(lds + troff + k * 4096)));
          const u32x2 p1 = __builtin_bit_cast(u32x2, __builtin_amdgcn_ds_read_tr16_b64_v4i16((LAS s16x4*)(lds + troff + k * 4096 + 512)));
          WB[k] = u32x4{p0[0], p0[1], p1[0], p1[1]};
        }
      }
      SB();
      bool ok0 = __all(g0[1] == tag && g0[3] == tag), ok1 = __all(g1[1] == tag && g1[3] == tag);
      for (u32 spins = 0; !(ok0 && ok1) && spins < SPIN_CAP; ++spins) {
        __builtin_amdgcn_s_sleep(ASLEEP);
        if (!ok0) g0 = ld_gran2(rs, off);
        if (!ok1) g1 = ld_gran2(rs, off + D8);
        ok0 = __all(g0[1] == tag && g0[3] == tag);
        ok1 = __all(g1[1] == tag && g1[3] == tag);
      }
      *(LAS u32x2*)(lds + L_OA + wave * OA_W + lane * 8) = u32x2{g0[0], g0[2]};
      *(LAS u32x2*)(lds + L_OA + (wave + 8) * OA_W + lane * 8) = u32x2{g1[0], g1[2]};
    }
    STAMP(7);
    u32x4 tg[3];
    u32 xw = 0;
    const int col = 64 * wave + 4 * li + lg;
    if (final_wg) {
#pragma unroll
      for (int k = 0; k < 3; ++k) tg[k] = ld_gran2(rs, WS_TB + (k * 512 + tid) * 16);
      xw = x[b * DM + col];
    }
    bar();
    if (wave >= 4) {
      f32x4 d0 = {0.f, 0.f, 0.f, 0.f}, d1 = {0.f, 0.f, 0.f, 0.f};
#pragma unroll
      for (int k = 0; k < 8; k += 2) {
        d0 = mfma32(*(const LAS u32x4*)(lds + L_OA + li * OA_W + k * 64 + lg * 16), WB[k], d0);
        d1 = mfma32(*(const LAS u32x4*)(lds + L_OA + li * OA_W + k * 64 + 64 + lg * 16), WB[k + 1], d1);
      }
      d0 += d1;
      const float o = (d0[0] + d0[1]) + (d0[2] + d0[3]);
      const float on = dppf<XOR1>(o);
      if ((lane & 1) == 0) st_gran(ws, WS_PB + (((b & 4) + lg) * 16 + kvh * 2 + kh) * 2048 + (32 * ct8 + 8 * (wave & 3) + (li >> 1)) * 8, tag, cvtpk(o, on));
    }
    STAMP(8);
    if (!final_wg) return;
    for (u32 spins = 0;; ++spins) {
      bool ok = true;
#pragma unroll
      for (int k = 0; k < 3; ++k) {
        ok &= tg[k][1] == tag && tg[k][3] == tag;
        *(LAS u32x2*)(lds + L_TAB + (k * 512 + tid) * 8) = u32x2{tg[k][0], tg[k][2]};
      }
      if (__all(ok) || spins > SPIN_CAP) break;
      __builtin_amdgcn_s_sleep(8);
#pragma unroll
      for (int k = 0; k < 3; ++k) tg[k] = ld_gran2(rs, WS_TB + (k * 512 + tid) * 16);
    }
    const float xv = bflo(xw);
    float a;
    for (u32 spins = 0;; ++spins) {
      u32x4 g[4];
#pragma unroll
      for (int q = 0; q < 4; ++q) g[q] = ld_gran2(rs, WS_PB + (b * 16 + 4 * q + lg) * 2048 + wave * 256 + li * 16);
      bool ok = true;
      float c0_ = 0.f, c1_ = 0.f, c2_ = 0.f, c3_ = 0.f;
#pragma unroll
      for (int q = 0; q < 4; ++q) {
        ok &= g[q][1] == tag && g[q][3] == tag;
        c0_ += bflo(g[q][0]); c1_ += bfhi(g[q][0]); c2_ += bflo(g[q][2]); c3_ += bfhi(g[q][2]);
      }
      const auto s0 = __builtin_amdgcn_permlane32_swap(__float_as_uint(c0_), __float_as_uint(c2_), false, false);
      const auto s1 = __builtin_amdgcn_permlane32_swap(__float_as_uint(c1_), __float_as_uint(c3_), false, false);
      const float e0 = __uint_as_float(s0[0]) + __uint_as_float(s0[1]), e1 = __uint_as_float(s1[0]) + __uint_as_float(s1[1]);
      const auto s2 = __builtin_amdgcn_permlane16_swap(__float_as_uint(e0), __float_as_uint(e1), false, false);
      a = __uint_as_float(s2[0]) + __uint_as_float(s2[1]);
      if (__all(ok) || spins > SPIN_CAP) break;
      __builtin_amdgcn_s_sleep(1);
    }
    STAMP(9);
    const float hv = rbf(xv + rbf(a));
    {
      const float s1 = wave_sum(hv), q1 = wave_sum(hv * hv);
      if (lane == 0) { *(LAS float*)(lds + L_HROW + wave * 4) = s1; *(LAS float*)(lds + L_HROW + 32 + wave * 4) = q1; }
    }
    bar();
    float tv;
    {
      const f32x4 sa = *(const LAS f32x4*)(lds + L_HROW), sb = *(const LAS f32x4*)(lds + L_HROW + 16);
      const f32x4 qa = *(const LAS f32x4*)(lds + L_HROW + 32), qb = *(const LAS f32x4*)(lds + L_HROW + 48);
      const f32x4 s4 = sa + sb, q4 = qa + qb;
      const float mean = ((s4[0] + s4[1]) + (s4[2] + s4[3])) * (1.0f / DM);
      const float var = fmaxf(fmaf(-mean, mean, ((q4[0] + q4[1]) + (q4[2] + q4[3])) * (1.0f / DM)), 0.f);
      const float mb1 = rbf(mean);
      const float inv1 = __builtin_amdgcn_rcpf(rbf(__builtin_amdgcn_sqrtf(rbf(rbf(var) + 1e-5f))));
      const float t1 = rbf(rbf(hv - mb1) * inv1);
      const float mb2 = rbf((mean - mb1) * inv1);
      const float inv2 = __builtin_amdgcn_rcpf(rbf(__builtin_amdgcn_sqrtf(rbf(rbf(var * inv1 * inv1) + 1e-5f))));
      tv = rbf(rbf(t1 - mb2) * inv2);
    }
    const u32 bits = __float_as_uint(tv) >> 16;
    const u32 rel = (bits & 0x7fffu) - (E_LO << 7);
    const bool in = rel < (u32)NT_HALF;
    float y = bflo(*(const LAS u16*)(lds + L_TAB + ((in ? rel : 0u) + (bits >> 15) * NT_HALF) * 2));
    u64 mm = __ballot(!in);
    while (mm) {
      const int src = __builtin_ctzll(mm);
      mm &= mm - 1;
      const float ts = __builtin_bit_cast(float, __builtin_amdgcn_readlane(__float_as_uint(tv), src));
      float acc = 0.f;
#pragma unroll 1
      for (int f = lane; f < FF; f += 64) acc = gelu_term(ts, bflo(w1[f]), bflo(b1[f]), bflo(w2[f]), acc);
      acc = wave_sum(acc);
      if (lane == src) y = rbf(rbf(acc) + bflo(b2[0]));
    }
    out[b * DM + col] = (u16)cvtpk(hv + y, 0.f);
    if (b == 0 && tid == 0) __hip_atomic_store((GAS u32*)(ws + WS_EPOCH), tag, RLX_AGENT);
    STAMP(10);
    return;
  }

  {
    const int tw = hwave - NSW - 1;
    __builtin_amdgcn_s_setprio(3);
    bar();
    SB();
    if (tw == 2) {
      const u32 t0 = (u32)__builtin_amdgcn_s_memrealtime();
      u32 pf = 0;
      {
        u64 pc; u32 back;
        asm volatile("s_getpc_b64 %0\n.Lpf_here:\n\ts_mov_b32 %1, .Lpf_here-fused_kernel" : "=s"(pc), "=s"(back));
        const u32 o = ((wg >> 3) * 8 + lane) * 128;
        if (lane < 8 && o + 4 <= CODE_BYTES) pf = *(const GAS u32*)(pc - back + o);
      }
      __builtin_amdgcn_s_sleep(TSLEEP);
      asm volatile("" ::: "memory");
      {
        u32x4 t[12];
        const int l3 = lane & 31;
#pragma unroll
        for (int c = 0; c < 4; ++c) {
          const int o = c * 64 + (c == 3 ? l3 : lane);
          t[c] = ((const u32x4*)w1)[o]; t[4 + c] = ((const u32x4*)b1)[o]; t[8 + c] = ((const u32x4*)w2)[o];
        }
#pragma unroll
        for (int c = 0; c < 12; ++c) *(LAS u32x4*)(lds + L_TW + (c >> 2) * 3584 + ((c & 3) * 64 + ((c & 3) == 3 ? l3 : lane)) * 16) = t[c];
        wait_lgkm();
        if (lane == 0) ctl[C_TW] = 1;
      }
      asm volatile("" ::"v"(pf));
      const u32x4* wb = (const u32x4*)wo + (size_t)(kvh * 512 + 256 * kh + (lane >> 3)) * 64 + 8 * ct8 + (lane & 7);
      u32x4 r[WOD];
#pragma unroll
      for (int i = 0; i < WOD; ++i) r[i] = u32x4{0u, 0u, 0u, 0u};
#pragma unroll 1
      for (int i0 = 0; i0 < 32; i0 += WOD) {
#pragma unroll
        for (int i = 0; i < WOD; ++i) {
          const int n = i0 + i;
          *(LAS u32x4*)(lds + L_WO + ((n - WOD) & 31) * 1024 + lane * 16) = r[i];
          while ((int)((u32)__builtin_amdgcn_s_memrealtime() - t0) < WO_T0 + n * WO_DT) __builtin_amdgcn_s_sleep(2);
          asm volatile("" ::: "memory");
          r[i] = ldp<W_NT>(wb + n * 512);
          SB();
        }
      }
#pragma unroll
      for (int i = 0; i < WOD; ++i) *(LAS u32x4*)(lds + L_WO + (32 - WOD + i) * 1024 + lane * 16) = r[i];
      wait_lgkm();
      STAMPW();
      STAMP(1);
      return;
    }
    const u32 tag = *(const CAS u32*)(ws + WS_EPOCH) + 1u;
    lds_wait_ge<8>(ctl + C_TW, 1);
    const float b2v = bflo(b2[0]);
    u32x2 r1[7], rb[7], r2[7];
#pragma unroll
    for (int c = 0; c < 7; ++c) {
      r1[c] = *(const LAS u32x2*)(lds + L_TW + (c * 64 + lane) * 8);
      rb[c] = *(const LAS u32x2*)(lds + L_TW + 3584 + (c * 64 + lane) * 8);
      r2[c] = *(const LAS u32x2*)(lds + L_TW + 7168 + (c * 64 + lane) * 8);
    }
    const int idx0 = wg * 24 + tw * 12;
    f32x2 f1[14], fb[14], f2[14];
#pragma unroll
    for (int c = 0; c < 14; ++c) {
      f1[c] = f32x2{bflo(r1[c >> 1][c & 1]), bfhi(r1[c >> 1][c & 1])};
      fb[c] = f32x2{bflo(rb[c >> 1][c & 1]), bfhi(rb[c >> 1][c & 1])};
      f2[c] = f32x2{bflo(r2[c >> 1][c & 1]), bfhi(r2[c >> 1][c & 1])};
    }
    __builtin_amdgcn_s_setprio(0);
    float res = 0.f;
#pragma unroll 1
    for (int e = 0; e < TAB_N; ++e) {
      const int i0 = idx0 + e;
      const float t0 = __uint_as_float((u32)((i0 >= NT_HALF ? i0 - NT_HALF + 0x8000 : i0) + (E_LO << 7)) << 16);
      const f32x2 tt = {t0, t0};
      f32x2 acc = {0.f, 0.f};
#pragma unroll
      for (int c0_ = 0; c0_ < 14; c0_ += 7) {
        f32x2 u[7], z[7];
#pragma unroll
        for (int c = 0; c < 7; ++c) u[c] = __builtin_elementwise_fma(tt, f1[c0_ + c], fb[c0_ + c]);
        SB();
#pragma unroll
        for (int c = 0; c < 7; ++c) z[c] = u[c] * u[c];
        SB();
#pragma unroll
        for (int c = 0; c < 7; ++c) z[c] = __builtin_elementwise_fma(z[c], f32x2{GK1, GK1}, f32x2{GK0, GK0});
        SB();
#pragma unroll
        for (int c = 0; c < 7; ++c) z[c] = z[c] * u[c];
        SB();
#pragma unroll
        for (int c = 0; c < 7; ++c) z[c] = f32x2{__builtin_amdgcn_exp2f(z[c][0]), __builtin_amdgcn_exp2f(z[c][1])};
        SB();
#pragma unroll
        for (int c = 0; c < 7; ++c) { z[c] = z[c] + f32x2{1.f, 1.f}; u[c] = u[c] * f2[c0_ + c]; }
        SB();
#pragma unroll
        for (int c = 0; c < 7; ++c) z[c] = f32x2{__builtin_amdgcn_rcpf(z[c][0]), __builtin_amdgcn_rcpf(z[c][1])};
        SB();
#pragma unroll
        for (int c = 0; c < 7; ++c) acc = __builtin_elementwise_fma(u[c], z[c], acc);
        SB();
      }
      const float a0 = wave_sum(acc[0] + acc[1]);
      if (lane == e) res = a0;
    }
    __builtin_amdgcn_s_setprio(3);
    {
      const float y = rbf(res) + b2v;
      const float yn = dppf<XOR1>(y);
      if (lane < 12 && (lane & 1) == 0) st_gran(ws, WS_TB + (idx0 + lane) * 4, tag, cvtpk(y, yn));
    }
    STAMPW();
    STAMP(1);
  }
}

extern "C" void kernel_launch(void* const* d_in, const int* in_sizes, int n_in, void* d_out, int out_size, void* d_ws, size_t ws_size,
                              hipStream_t stream) {
  fused_kernel<<<dim3(NWG), dim3(NTHR), 0, stream>>>((const u16*)d_in[0], (const u16*)d_in[1], (const u16*)d_in[2], (const u16*)d_in[3],
                                                     (const u16*)d_in[4], (const u16*)d_in[6], (const u16*)d_in[7], (const u16*)d_in[8],
                                                     (const u16*)d_in[9], (u16*)d_out, (unsigned char*)d_ws);
}
```

```cpp
#include <hip/hip_runtime.h>
#include <stdint.h>
#include <utility>
#include <type_traits>

typedef unsigned short u16;
typedef unsigned u32;
typedef unsigned long long u64;
typedef u32 u32x2 __attribute__((ext_vector_type(2)));
typedef u32 u32x4 __attribute__((ext_vector_type(4)));
typedef float f32x2 __attribute__((ext_vector_type(2)));
typedef float f32x4 __attribute__((ext_vector_type(4)));
typedef short s16x4 __attribute__((ext_vector_type(4)));
typedef __bf16 bf16x8 __attribute__((ext_vector_type(8)));
#define LAS __attribute__((address_space(3)))
#define GAS __attribute__((address_space(1)))
#define RLX_AGENT __ATOMIC_RELAXED, __HIP_MEMORY_SCOPE_AGENT

constexpr int PACE = 28;
constexpr int LATE1 = 50, LATE2 = 150, LATE3 = 250;
constexpr int WCAP0 = 3, WCAP1 = 4, WCAP2 = 5, WCAP = 14;
constexpr int WIN0 = 3;
constexpr int XV = 4;
constexpr int LAG = 12;
constexpr int QTRIG = 16, MLTRIG = 54;
constexpr int ADELAY = 26, BDELAY = 10;
constexpr int TSLEEP = 60;
constexpr int CODE_BYTES = 16384;

constexpr int DM = 512, HKV = 8, SK = 4096, FF = 1792;
constexpr int NWG = 256, NSW = 8, NTHR = 768;
constexpr int NE = 24, E_LO = 132 - NE, NT_HALF = NE * 128, NTAB = 2 * NT_HALF;
static_assert(NTAB == NWG * 24, "24 table entries per workgroup");
constexpr float SCALE = 0.08838834764831845f, LOG2E = 1.4426950408889634f;
constexpr float GK0 = -2.302208198f, GK1 = GK0 * 0.044715f;
constexpr u32 SPIN_CAP = 1u << 16, LSPIN_CAP = 1u << 22;

constexpr u32 WS_EPOCH = 0, WS_QG = 4096, WS_ML = WS_QG + 1048576, WS_TB = WS_ML + 16384, WS_END = WS_TB + NTAB * 4;
constexpr u32 L_QF = 0, L_CTL = 1024, L_MX = 1088, L_LW = 1216, L_FS = 1344, L_HROW = 1536, L_OA = 2048, OA_W = 528, L_RED = L_OA + 16 * OA_W,
              L_TAB = L_RED + 16384, L_WQ = L_TAB + NTAB * 2, L_TW = L_WQ + 16384, L_WO = L_TW + 10752, L_XROW = L_WO + 32768, L_END = L_XROW + 1024;
enum { C_Q = 0, C_ML = 1, C_QTRIG = 2, C_MLTRIG = 3, C_TAG = 4, C_TW = 5, C_WOTRIG = 6, C_WO = 7 };

__device__ __forceinline__ float bflo(u32 w) { return __builtin_bit_cast(float, w << 16); }
__device__ __forceinline__ float bfhi(u32 w) { return __builtin_bit_cast(float, w & 0xffff0000u); }
__device__ __forceinline__ u32 cvtpk(float lo, float hi) {
  typedef __bf16 bf16x2 __attribute__((ext_vector_type(2)));
  bf16x2 r = {(__bf16)lo, (__bf16)hi};
  return __builtin_bit_cast(u32, r);
}
__device__ __forceinline__ float rbf(float f) {
  float z;
  asm("s_mov_b32 %0, 0" : "=s"(z));
  return __uint_as_float(cvtpk(z, f));
}
template <int CTRL> __device__ __forceinline__ float dppf(float x) {
  return __builtin_bit_cast(float, __builtin_amdgcn_mov_dpp(__builtin_bit_cast(int, x), CTRL, 0xf, 0xf, true));
}
template <int CTRL> __device__ __forceinline__ u32 dppu(u32 x) { return (u32)__builtin_amdgcn_mov_dpp((int)x, CTRL, 0xf, 0xf, true); }
constexpr int XOR1 = 0xB1, XOR2 = 0x4E, HMIR = 0x141, MIR = 0x140, ROR8 = 0x128;
__device__ __forceinline__ float xrow_sum(float x) {
  auto s = __builtin_amdgcn_permlane16_swap(__float_as_uint(x), __float_as_uint(x), false, false);
  x = __uint_as_float(s[0]) + __uint_as_float(s[1]);
  auto t = __builtin_amdgcn_permlane32_swap(__float_as_uint(x), __float_as_uint(x), false, false);
  return __uint_as_float(t[0]) + __uint_as_float(t[1]);
}
__device__ __forceinline__ float xrow_max(float x) {
  auto s = __builtin_amdgcn_permlane16_swap(__float_as_uint(x), __float_as_uint(x), false, false);
  x = fmaxf(__uint_as_float(s[0]), __uint_as_float(s[1]));
  auto t = __builtin_amdgcn_permlane32_swap(__float_as_uint(x), __float_as_uint(x), false, false);
  return fmaxf(__uint_as_float(t[0]), __uint_as_float(t[1]));
}
__device__ __forceinline__ float wave_sum(float x) {
  x += dppf<XOR1>(x); x += dppf<XOR2>(x); x += dppf<HMIR>(x); x += dppf<MIR>(x);
  return xrow_sum(x);
}
__device__ __forceinline__ f32x4 mfma32(u32x4 a, u32x4 b, f32x4 c) {
  return __builtin_amdgcn_mfma_f32_16x16x32_bf16(__builtin_bit_cast(bf16x8, a), __builtin_bit_cast(bf16x8, b), c, 0, 0, 0);
}
__device__ __forceinline__ f32x4 mfma16(u32x2 a, u32x2 b, f32x4 c) {
  return __builtin_amdgcn_mfma_f32_16x16x16bf16_1k(__builtin_bit_cast(s16x4, a), __builtin_bit_cast(s16x4, b), c, 0, 0, 0);
}
__device__ __forceinline__ void wait_lgkm() { asm volatile("s_waitcnt lgkmcnt(0)" ::: "memory"); }
__device__ __forceinline__ void bar() { asm volatile("s_waitcnt lgkmcnt(0)\n\ts_barrier" ::: "memory"); }
#define SB() __builtin_amdgcn_sched_barrier(0)
#define IC(n) std::integral_constant<int, (n)>{}
template <int L, typename F, int... I> __device__ __forceinline__ void for_impl(F&& f, std::integer_sequence<int, I...>) { (f(std::integral_constant<int, L + I>{}), ...); }
template <int L, int H, typename F> __device__ __forceinline__ void for_range(F&& f) { for_impl<L>(f, std::make_integer_sequence<int, (H > L ? H - L : 0)>{}); }
__device__ __forceinline__ u32x4 ldnt(const u32x4* p) { return __builtin_nontemporal_load(p); }
__device__ __forceinline__ void st_gran(unsigned char* ws, u32 off, u32 tag, u32 val) {
  __hip_atomic_store((GAS u64*)(ws + off), ((u64)tag << 32) | val, RLX_AGENT);
}
__device__ __forceinline__ u32x4 ld_gran2(__amdgpu_buffer_rsrc_t rs, u32 off) { return __builtin_amdgcn_raw_buffer_load_b128(rs, off, 0, 16); }
#define PIN() asm volatile("" ::: "memory")
__device__ __forceinline__ u32 next_tag(u32 t) { return (t + 1u) * 0x9E3779B1u; }
__device__ __forceinline__ void pin2(f32x4& a, f32x4& b) { asm volatile("" : "+v"(a), "+v"(b)); }
__device__ __forceinline__ void pace_call(u32& due, u64 psub) {
  asm volatile("s_swappc_b64 s[92:93], %1" : "+{s96}"(due) : "s"(psub) : "s92", "s93", "s94", "s95", "scc", "memory");
}
template <int SL> __device__ __forceinline__ void lds_wait_ge(volatile LAS u32* w, u32 n) {
  for (u32 spins = 0; *w < n && spins < LSPIN_CAP; ++spins) __builtin_amdgcn_s_sleep(SL);
  asm volatile("" ::: "memory");
}

__device__ __forceinline__ float gelu_term(float t, float w1, float b1, float w2, float acc) {
  const float u = fmaf(t, w1, b1);
  const float z = u * fmaf(u * u, GK1, GK0);
  const float e = __builtin_amdgcn_exp2f(z);
  return fmaf(u * w2, __builtin_amdgcn_rcpf(1.0f + e), acc);
}
constexpr int vdone(int n) {
  const int a = 2 * (n - (40 + XV)), c = n - 32 - LAG;
  return (a < 0 || c < 0) ? 0 : (a < c ? a : c);
}

extern "C" __global__ void __launch_bounds__(NTHR, 3) fused_kernel(const u16* x, const u16* kc, const u16* vc, const u16* wq, const u16* wo,
                                                                const u16* w1, const u16* b1, const u16* w2, const u16* b2, u16* out,
                                                                unsigned char* ws) {
  __shared__ __attribute__((aligned(16))) unsigned char lds[L_END];
  const int lane = threadIdx.x & 63, hwave = __builtin_amdgcn_readfirstlane(threadIdx.x >> 6);
  const int wg = blockIdx.x, kvh = wg & 7, sp = (wg >> 3) & 3, b = wg >> 5;
  const bool final_wg = (kvh == b) && (sp == 0);
  const int li = lane & 15, lg = lane >> 4;
  volatile LAS u32* ctl = (volatile LAS u32*)(lds + L_CTL);
  const u32 grp_off = (u32)(b * HKV + kvh);
  const int kh = (b >> 1) & 1, ct8 = (b & 1) * 4 + sp;

  if (threadIdx.x < 8) ctl[threadIdx.x] = 0;
  bar();
  SB();
  if (__builtin_expect(hwave >= 1 && hwave <= NSW, 1)) {
    const int wave = hwave - 1, tid = threadIdx.x - 64;
    const size_t tile = ((size_t)grp_off * SK + sp * 1024) * 16;
    const u32x4* kb = (const u32x4*)kc + tile + wave * 256;
    const u32x4* vb = (const u32x4*)vc + tile + wave * 256;
    const u32 klane = ((lane >> 2) & 3) * 16 + 4 * lg + (lane & 3);
    u32x4 K[32], V[32];
    auto issue = [&](auto nc) {
      constexpr int n = decltype(nc)::value;
      if constexpr (n < 32) K[n] = ldnt(kb + (n >> 2) * 2048 + (n & 3) * 64 + klane);
      else V[n - 32] = ldnt(vb + ((n - 32) >> 2) * 2048 + (n & 3) * 64 + lane);
    };
    for_range<0, WIN0>([&](auto n) { issue(n); });
    SB();
    u32 due = (u32)__builtin_amdgcn_s_memrealtime() + (u32)(WIN0 * PACE + wave * PACE / 8);
    u64 psub;
    asm volatile(
        "s_getpc_b64 %0\n\t"
        "s_branch .Lpace_end\n"
        ".Lpace_sub:\n\t"
        "s_memrealtime s[94:95]\n\t"
        "s_waitcnt vmcnt(%1) lgkmcnt(0)\n\t"
        "s_sub_i32 s94, s94, s96\n\t"
        "s_cmp_gt_i32 s94, %2\n\t"
        "s_cbranch_scc1 .Lpace_late\n\t"
        "s_setprio 0\n\t"
        "s_waitcnt vmcnt(%5)\n\t"
        "s_cmp_ge_i32 s94, 0\n\t"
        "s_cbranch_scc1 .Lpace_ret\n\t"
        "s_sleep 1\n\t"
        "s_branch .Lpace_sub\n"
        ".Lpace_late:\n\t"
        "s_setprio 2\n\t"
        "s_cmp_gt_i32 s94, %7\n\t"
        "s_cbranch_scc1 .Lpace_ret\n\t"
        "s_waitcnt vmcnt(%8)\n\t"
        "s_cmp_gt_i32 s94, %3\n\t"
        "s_cbranch_scc1 .Lpace_ret\n\t"
        "s_setprio 1\n\t"
        "s_waitcnt vmcnt(%6)\n"
        ".Lpace_ret:\n\t"
        "s_add_i32 s96, s96, %4\n\t"
        "s_setpc_b64 s[92:93]\n"
        ".Lpace_end:\n\t"
        : "=s"(psub)
        : "n"(WCAP - 1), "n"(LATE1), "n"(LATE2), "n"(PACE), "n"(WCAP0 - 1), "n"(WCAP1 - 1), "n"(LATE3), "n"(WCAP2 - 1));
    psub += 4;
    auto pace = [&](auto) { pace_call(due, psub); };
    for_range<WIN0, 32 + XV>([&](auto nc) {
      constexpr int n = decltype(nc)::value;
      pace(nc);
      SB();
      issue(nc);
      if constexpr (n == QTRIG) ctl[C_QTRIG] = 1;
      SB();
    });
    lds_wait_ge<1>(ctl + C_Q, 1);
    const u32 tag = __builtin_amdgcn_readfirstlane(ctl[C_TAG]);
    const __amdgpu_buffer_rsrc_t rs = __builtin_amdgcn_make_buffer_rsrc(ws, 0, WS_END, 0x00020000);
    const u32x4 qf = *(const LAS u32x4*)(lds + L_QF + (li >> 2) * 256 + lg * 64 + (li & 3) * 16);
    const bool c0 = lane & 1, c1 = lane & 2;
    float S[8];
    u32 pp[8];
    f32x4 acc0 = {0.f, 0.f, 0.f, 0.f}, acc1 = {0.f, 0.f, 0.f, 0.f};
    const u32 half = c0 ? 0xffff0000u : 0x0000ffffu;
    const u32 mask0 = c1 ? 0u : half, mask1 = c1 ? half : 0u;
    auto consume_kgroup = [&](auto uc) {
      constexpr int u = decltype(uc)::value;
      const f32x4 z = {0.f, 0.f, 0.f, 0.f};
      float v[4];
#pragma unroll
      for (int t = 0; t < 4; ++t) {
        const f32x4 d = mfma32(K[4 * u + t], qf, z);
        const float lo = c0 ? d[1] : d[0], hi = c0 ? d[3] : d[2];
        v[t] = c1 ? hi : lo;
      }
      const float w0 = (c0 ? v[1] : v[0]) + dppf<XOR1>(c0 ? v[0] : v[1]);
      const float w1_ = (c0 ? v[3] : v[2]) + dppf<XOR1>(c0 ? v[2] : v[3]);
      S[u] = rbf(rbf((c1 ? w1_ : w0) + dppf<XOR2>(c1 ? w0 : w1_)) * SCALE);
    };
    auto softmax = [&]() {
      float m = S[0];
#pragma unroll
      for (int u = 1; u < 8; ++u) m = fmaxf(m, S[u]);
      m = fmaxf(m, dppf<XOR1>(m)); m = fmaxf(m, dppf<XOR2>(m)); m = xrow_max(m);
      float l = 0.f;
#pragma unroll
      for (int u = 0; u < 8; ++u) {
        const float p = __builtin_amdgcn_exp2f((S[u] - m) * LOG2E);
        pp[u] = cvtpk(p, p);
        l += bflo(pp[u]);
      }
      l += dppf<XOR1>(l); l += dppf<XOR2>(l); l = xrow_sum(l);
      if ((lane & 3) == 0 && lg == 0) {
        *(LAS float*)(lds + L_MX + ((li >> 2) * 8 + wave) * 4) = m;
        *(LAS float*)(lds + L_LW + ((li >> 2) * 8 + wave) * 4) = l;
      }
      wait_lgkm();
      if (lane == 0) __hip_atomic_fetch_add((LAS u32*)(lds + L_CTL + 4 * C_ML), 1u, __ATOMIC_RELAXED, __HIP_MEMORY_SCOPE_WORKGROUP);
    };
    auto consume_v = [&](auto vc_) {
      constexpr int v = decltype(vc_)::value, u = v >> 2, t = v & 3;
      const u32 pb = dppu<t * 0x55>(pp[u]);
      const u32x2 a = {pb & mask0, pb & mask1};
      acc0 = mfma16(a, u32x2{V[v][0], V[v][1]}, acc0);
      acc1 = mfma16(a, u32x2{V[v][2], V[v][3]}, acc1);
      pin2(acc0, acc1);
    };
    constexpr int N1 = 32 + XV, N2 = N1 + 8;
    for_range<N1, 64>([&](auto nc) {
      constexpr int n = decltype(nc)::value;
      if constexpr (n < N2) consume_kgroup(IC(n - N1));
      if constexpr (n == N2) { softmax(); }
      if constexpr (n > N2) for_range<vdone(n - 1), vdone(n)>([&](auto v) { consume_v(v); });
      SB();
      pace(nc);
      SB();
      issue(nc);
      if constexpr (n == MLTRIG) ctl[C_MLTRIG] = 1;
      if constexpr (n == 63) { if (lane == 0) __hip_atomic_fetch_add((LAS u32*)(lds + L_CTL + 4 * C_WOTRIG), 1u, __ATOMIC_RELAXED, __HIP_MEMORY_SCOPE_WORKGROUP); }
      SB();
    });
    u32x4 tg[3];
    const u32 tgo = WS_TB + tid * 16 + (final_wg ? 0u : 0x40000000u);
#pragma unroll
    for (int k = 0; k < 3; ++k) tg[k] = ld_gran2(rs, tgo + k * 8192);
    if (wave < 4) __builtin_amdgcn_s_setprio(3); else __builtin_amdgcn_s_setprio(0);
    u32 pa_off = WS_QG + grp_off * 16384 + sp * 2048 + tid * 8;
    u32 pb_off = WS_QG + (((b & 4) + lg) * HKV + kvh) * 16384 + 8192 + kh * 2048 + (32 * ct8 + 8 * (wave & 3) + (li >> 1)) * 8;
    u32 ga_off = WS_QG + (((b & 4) + (wave >> 2)) * HKV + kvh) * 16384 + (wave & 3) * 2048 + kh * 1024 + lane * 16;
    const u32 tag2 = next_tag(tag);
    u32 oa_rd = L_OA + li * OA_W + lg * 16, oa_wr = L_OA + wave * OA_W + lane * 8, red_rd = L_RED + tid * 8, red_wr = L_RED + wave * 2048 + lane * 32;
    asm volatile("" : "+v"(pa_off), "+v"(pb_off), "+v"(ga_off), "+v"(oa_rd), "+v"(oa_wr), "+v"(red_rd), "+v"(red_wr));
    SB();
    for_range<vdone(63), 32>([&](auto v) { consume_v(v); SB(); });
    *(LAS f32x4*)(lds + red_wr) = acc0;
    *(LAS f32x4*)(lds + red_wr + 16) = acc1;
    bar();
    if (wave < 4) {
      const f32x4 fa = *(const LAS f32x4*)(lds + L_FS + wave * 32), fb = *(const LAS f32x4*)(lds + L_FS + wave * 32 + 16);
      f32x2 r[8];
#pragma unroll
      for (int w = 0; w < 8; ++w) r[w] = *(const LAS f32x2*)(lds + red_rd + w * 2048);
      SB();
      f32x2 s0 = r[0] * fa[0], s1 = r[4] * fb[0];
#pragma unroll
      for (int w = 1; w < 4; ++w) { s0 += r[w] * fa[w]; s1 += r[4 + w] * fb[w]; }
      s0 += s1;
      st_gran(ws, pa_off, tag2, cvtpk(s0[0], s0[1]));
    }
    u32x4 WB[8];
    {
      const u32 off = ga_off;
      constexpr u32 D8 = 2 * HKV * 16384;
      SB();
      if (final_wg) {
        for (u32 spins = 0;; ++spins) {
          bool ok = true;
#pragma unroll
          for (int k = 0; k < 3; ++k) {
            ok &= tg[k][1] == tag && tg[k][3] == tag;
            *(LAS u32x2*)(lds + L_TAB + (k * 512 + tid) * 8) = u32x2{tg[k][0], tg[k][2]};
          }
          if (__all(ok) || spins > SPIN_CAP) break;
          __builtin_amdgcn_s_sleep(8);
          PIN();
#pragma unroll
          for (int k = 0; k < 3; ++k) tg[k] = ld_gran2(rs, tgo + k * 8192);
        }
      }
      __builtin_amdgcn_s_sleep(ADELAY);
      asm volatile("" ::: "memory");
      u32x4 g0 = ld_gran2(rs, off), g1 = ld_gran2(rs, off + D8);
      PIN();
      if (wave >= 4) {
        lds_wait_ge<1>(ctl + C_WO, 1);
        const u32 troff = L_WO + (8 * lg + (li >> 2)) * 128 + (wave & 3) * 32 + (li & 3) * 8;
#pragma unroll
        for (int k = 0; k < 8; ++k) {
          const u32x2 p0 = __builtin_bit_cast(u32x2, __builtin_amdgcn_ds_read_tr16_b64_v4i16((LAS s16x4*)(lds + troff + k * 4096)));
          const u32x2 p1 = __builtin_bit_cast(u32x2, __builtin_amdgcn_ds_read_tr16_b64_v4i16((LAS s16x4*)(lds + troff + k * 4096 + 512)));
          WB[k] = u32x4{p0[0], p0[1], p1[0], p1[1]};
        }
      }
      PIN();
      bool ok0 = __all(g0[1] == tag2 && g0[3] == tag2), ok1 = __all(g1[1] == tag2 && g1[3] == tag2);
#pragma unroll 1
      for (u32 spins = 0; !(ok0 && ok1) && spins < SPIN_CAP; ++spins) {
        PIN();
        if (!ok0) g0 = ld_gran2(rs, off);
        if (!ok1) g1 = ld_gran2(rs, off + D8);
        ok0 = __all(g0[1] == tag2 && g0[3] == tag2);
        ok1 = __all(g1[1] == tag2 && g1[3] == tag2);
      }
      *(LAS u32x2*)(lds + oa_wr) = u32x2{g0[0], g0[2]};
      *(LAS u32x2*)(lds + oa_wr + 8 * OA_W) = u32x2{g1[0], g1[2]};
    }
    bar();
    if (wave >= 4) {
      u32x4 A[8];
#pragma unroll
      for (int k = 0; k < 8; ++k) A[k] = *(const LAS u32x4*)(lds + oa_rd + k * 64);
      SB();
      f32x4 d0 = {0.f, 0.f, 0.f, 0.f}, d1 = {0.f, 0.f, 0.f, 0.f};
#pragma unroll
      for (int k = 0; k < 8; k += 2) {
        d0 = mfma32(A[k], WB[k], d0);
        d1 = mfma32(A[k + 1], WB[k + 1], d1);
      }
      d0 += d1;
      const float o = (d0[0] + d0[1]) + (d0[2] + d0[3]);
      const float on = dppf<XOR1>(o);
      if ((lane & 1) == 0) st_gran(ws, pb_off, tag2, cvtpk(o, on));
    }
    if (!final_wg) return;
    const int col = 64 * wave + 4 * li + lg;
    const float xv = bflo(*(const LAS u16*)(lds + L_XROW + col * 2)), piv = *(const LAS float*)(lds + L_HROW + 64);
    float a;
    {
      const u32 off = WS_QG + (b * HKV + (lg >> 1)) * 16384 + 8192 + (lg & 1) * 2048 + wave * 256 + li * 16;
      u32x4 g[4];
      __builtin_amdgcn_s_sleep(BDELAY);
#pragma unroll 1
      for (u32 spins = 0;; ++spins) {
        PIN();
#pragma unroll
        for (int q = 0; q < 4; ++q) g[q] = ld_gran2(rs, off + q * 32768);
        bool ok = true;
#pragma unroll
        for (int q = 0; q < 4; ++q) ok &= g[q][1] == tag2 && g[q][3] == tag2;
        if (__all(ok) || spins > SPIN_CAP) break;
      }
      const float c0_ = (bflo(g[0][0]) + bflo(g[1][0])) + (bflo(g[2][0]) + bflo(g[3][0])), c1_ = (bfhi(g[0][0]) + bfhi(g[1][0])) + (bfhi(g[2][0]) + bfhi(g[3][0]));
      const float c2_ = (bflo(g[0][2]) + bflo(g[1][2])) + (bflo(g[2][2]) + bflo(g[3][2])), c3_ = (bfhi(g[0][2]) + bfhi(g[1][2])) + (bfhi(g[2][2]) + bfhi(g[3][2]));
      const auto s0 = __builtin_amdgcn_permlane32_swap(__float_as_uint(c0_), __float_as_uint(c2_), false, false);
      const auto s1 = __builtin_amdgcn_permlane32_swap(__float_as_uint(c1_), __float_as_uint(c3_), false, false);
      const float e0 = __uint_as_float(s0[0]) + __uint_as_float(s0[1]), e1 = __uint_as_float(s1[0]) + __uint_as_float(s1[1]);
      const auto s2 = __builtin_amdgcn_permlane16_swap(__float_as_uint(e0), __float_as_uint(e1), false, false);
      a = __uint_as_float(s2[0]) + __uint_as_float(s2[1]);
    }
    const float hv = rbf(xv + rbf(a));
    *(LAS u16*)(lds + L_XROW + col * 2) = (u16)(__float_as_uint(hv) >> 16);
    {
      const float hd = hv - piv, s1 = wave_sum(hd), q1 = wave_sum(hd * hd);
      if (lane == 0) { *(LAS float*)(lds + L_HROW + wave * 4) = s1; *(LAS float*)(lds + L_HROW + 32 + wave * 4) = q1; }
    }
    bar();
    if (wave < 4) {
      const f32x4 sa = *(const LAS f32x4*)(lds + L_HROW), sb = *(const LAS f32x4*)(lds + L_HROW + 16);
      const f32x4 qa = *(const LAS f32x4*)(lds + L_HROW + 32), qb = *(const LAS f32x4*)(lds + L_HROW + 48);
      const u32 hp = *(const LAS u32*)(lds + L_XROW + tid * 4);
      const f32x4 s4 = sa + sb, q4 = qa + qb;
      const float dm = ((s4[0] + s4[1]) + (s4[2] + s4[3])) * (1.0f / DM), mean = piv + dm;
      const float var = fmaxf(fmaf(-dm, dm, ((q4[0] + q4[1]) + (q4[2] + q4[3])) * (1.0f / DM)), 0.f);
      const float mb1 = rbf(mean);
      const float inv1 = __builtin_amdgcn_rcpf(rbf(__builtin_amdgcn_sqrtf(rbf(rbf(var) + 1e-5f))));
      const float mb2 = rbf((mean - mb1) * inv1);
      const float inv2 = __builtin_amdgcn_rcpf(rbf(__builtin_amdgcn_sqrtf(rbf(rbf(var * inv1 * inv1) + 1e-5f))));
      float hh[2] = {bflo(hp), bfhi(hp)}, tv[2], y[2];
      bool in[2];
#pragma unroll
      for (int e = 0; e < 2; ++e) {
        tv[e] = rbf(rbf(rbf(rbf(hh[e] - mb1) * inv1) - mb2) * inv2);
        const u32 bits = __float_as_uint(tv[e]) >> 16;
        const u32 rel = (bits & 0x7fffu) - (E_LO << 7);
        in[e] = rel < (u32)NT_HALF;
        y[e] = bflo(*(const LAS u16*)(lds + L_TAB + ((in[e] ? rel : 0u) + (bits >> 15) * NT_HALF) * 2));
      }
      for (;;) {
        const u64 m0 = __ballot(!in[0]), m1 = __ballot(!in[1]);
        if ((m0 | m1) == 0) break;
        const u32 tb = m0 ? __builtin_amdgcn_readlane(__float_as_uint(tv[0]), __builtin_ctzll(m0)) : __builtin_amdgcn_readlane(__float_as_uint(tv[1]), __builtin_ctzll(m1));
        float acc = 0.f;
#pragma unroll 1
        for (int f = lane; f < FF; f += 64)
          acc = gelu_term(__uint_as_float(tb), bflo(*(const LAS u16*)(lds + L_TW + f * 2)), bflo(*(const LAS u16*)(lds + L_TW + 3584 + f * 2)),
                          bflo(*(const LAS u16*)(lds + L_TW + 7168 + f * 2)), acc);
        acc = rbf(rbf(wave_sum(acc)) + bflo(b2[0]));
        if (__float_as_uint(tv[0]) == tb) { y[0] = acc; in[0] = true; }
        if (__float_as_uint(tv[1]) == tb) { y[1] = acc; in[1] = true; }
      }
      ((u32*)out)[b * (DM / 2) + tid] = cvtpk(hh[0] + y[0], hh[1] + y[1]);
    }
    if (b == 0 && tid == 0) __hip_atomic_store((GAS u32*)(ws + WS_EPOCH), tag2, RLX_AGENT);
    return;
  }

  if (__builtin_expect(hwave == 0, 1)) {
    __builtin_amdgcn_s_setprio(3);
    const int cb = ((wg >> 3) + 4) & 7, kq = wg >> 6;
    u32x4 xa[8], wt[16];
    const u32x4* xp = (const u32x4*)x + (li & 7) * 64 + (li >> 3) * 4 + lg;
    const u32x4* wp = (const u32x4*)(wq + (size_t)(128 * kq) * 4096 + kvh * 512 + 64 * cb) + (lane >> 3) * 512 + (lane & 7);
#pragma unroll
    for (int k = 0; k < 8; ++k) xa[k] = xp[((k + 2 * kq) & 7) * 8];
#pragma unroll
    for (int k = 0; k < 16; ++k) wt[k] = wp[k * 8 * 512];
    const u32 epoch = __hip_atomic_load((GAS u32*)(ws + WS_EPOCH), RLX_AGENT);
    SB();
    const __amdgpu_buffer_rsrc_t rs = __builtin_amdgcn_make_buffer_rsrc(ws, 0, WS_END, 0x00020000);
    if ((li & 7) == b) {
#pragma unroll
      for (int k = 0; k < 8; ++k) *(LAS u32x4*)(lds + L_XROW + (((k + 2 * kq) & 7) * 8 + (li >> 3) * 4 + lg) * 16) = xa[k];
    }
    {
      const float piv = __shfl(bflo(xa[0][0]), li & 7);
      f32x2 sm = {0.f, 0.f}, sq = {0.f, 0.f};
#pragma unroll
      for (int k = 0; k < 8; ++k)
#pragma unroll
        for (int e = 0; e < 4; ++e) {
          const f32x2 v = f32x2{bflo(xa[k][e]), bfhi(xa[k][e])} - f32x2{piv, piv};
          sm += v;
          sq = __builtin_elementwise_fma(v, v, sq);
        }
      float s1 = sm[0] + sm[1], s2 = sq[0] + sq[1];
      s1 += dppf<ROR8>(s1); s2 += dppf<ROR8>(s2);
      const float dm = xrow_sum(s1) * (1.0f / DM);
      const float var = rbf(fmaxf(fmaf(-dm, dm, xrow_sum(s2) * (1.0f / DM)), 0.f)), mb = rbf(piv + dm);
      if (lane == b) *(LAS float*)(lds + L_HROW + 64) = mb;
      const float inv = __builtin_amdgcn_rcpf(rbf(__builtin_amdgcn_sqrtf(rbf(var + 1e-5f))));
#pragma unroll
      for (int k = 0; k < 2; ++k)
#pragma unroll
        for (int e = 0; e < 4; ++e) {
          const u32 d = cvtpk(bflo(xa[k][e]) - mb, bfhi(xa[k][e]) - mb);
          xa[k][e] = cvtpk(bflo(d) * inv, bfhi(d) * inv);
        }
    }
    SB();
#pragma unroll
    for (int k = 0; k < 16; ++k) *(LAS u32x4*)(lds + L_WQ + k * 1024 + lane * 16) = wt[k];
    wait_lgkm();
    const u32 tag = next_tag(__builtin_amdgcn_readfirstlane(epoch));
    const u32 troff = L_WQ + (8 * lg + (li >> 2)) * 128 + (li & 3) * 8;
    const bool odd = lane & 1, up = lg & 2;
    const u32 qoff = WS_QG + ((4 * (lg & 1) + (lg & 2) + (lane & 1)) * HKV + kvh) * 16384 + kq * 4096 + (64 * cb + (li & 14)) * 8;
#pragma unroll
    for (int ct = 0; ct < 4; ++ct) {
      f32x4 d0 = {0.f, 0.f, 0.f, 0.f}, d1 = {0.f, 0.f, 0.f, 0.f};
#pragma unroll
      for (int k = 0; k < 4; ++k) {
        const s16x4 b0 = __builtin_amdgcn_ds_read_tr16_b64_v4i16((LAS s16x4*)(lds + troff + k * 4096 + ct * 32));
        const s16x4 b1_ = __builtin_amdgcn_ds_read_tr16_b64_v4i16((LAS s16x4*)(lds + troff + k * 4096 + ct * 32 + 512));
        const u32x2 p0 = __builtin_bit_cast(u32x2, b0), p1 = __builtin_bit_cast(u32x2, b1_);
        if (k & 1) d1 = mfma32(xa[k >> 1], u32x4{p0[0], p0[1], p1[0], p1[1]}, d1);
        else d0 = mfma32(xa[k >> 1], u32x4{p0[0], p0[1], p1[0], p1[1]}, d0);
      }
      const auto s0 = __builtin_amdgcn_permlane32_swap(__float_as_uint(d1[0]), __float_as_uint(d0[2]), false, false);
      const auto s1 = __builtin_amdgcn_permlane32_swap(__float_as_uint(d1[1]), __float_as_uint(d0[3]), false, false);
      const float e0 = up ? d1[2] + __uint_as_float(s0[0]) : d0[0] + __uint_as_float(s0[1]);
      const float e1 = up ? d1[3] + __uint_as_float(s1[0]) : d0[1] + __uint_as_float(s1[1]);
      const float mine = odd ? e1 : e0, got = dppf<XOR1>(odd ? e0 : e1);
      const u32x4 g = {__float_as_uint(odd ? got : mine), tag, __float_as_uint(odd ? mine : got), tag};
      __builtin_amdgcn_raw_buffer_store_b128(g, rs, qoff + ct * 128, 0, 16);
    }
    lds_wait_ge<2>(ctl + C_QTRIG, 1);
    {
      const u32 off = WS_QG + grp_off * 16384 + lane * 16;
      for (u32 spins = 0;; ++spins) {
        u32x4 g[16];
#pragma unroll
        for (int k = 0; k < 16; ++k) g[k] = ld_gran2(rs, off + k * 1024);
        bool ok = true;
#pragma unroll
        for (int k = 0; k < 16; ++k) ok &= g[k][1] == tag && g[k][3] == tag;
#pragma unroll
        for (int h = 0; h < 4; ++h) {
          const float q0 = (__uint_as_float(g[h][0]) + __uint_as_float(g[4 + h][0])) + (__uint_as_float(g[8 + h][0]) + __uint_as_float(g[12 + h][0]));
          const float q1 = (__uint_as_float(g[h][2]) + __uint_as_float(g[4 + h][2])) + (__uint_as_float(g[8 + h][2]) + __uint_as_float(g[12 + h][2]));
          *(LAS u32*)(lds + L_QF + (h * 64 + lane) * 4) = cvtpk(q0, q1);
        }
        if (__all(ok) || spins > SPIN_CAP) break;
        __builtin_amdgcn_s_sleep(8);
        PIN();
      }
      if (lane == 0) ctl[C_TAG] = tag;
      wait_lgkm();
      if (lane == 0) ctl[C_Q] = 1;
    }
    lds_wait_ge<4>(ctl + C_ML, NSW);

    {
      const float mw = *(const LAS float*)(lds + L_MX + (lane & 31) * 4), lw = *(const LAS float*)(lds + L_LW + (lane & 31) * 4);
      float M = fmaxf(mw, dppf<XOR1>(mw)); M = fmaxf(M, dppf<XOR2>(M)); M = fmaxf(M, dppf<HMIR>(M));
      float l = lw * __builtin_amdgcn_exp2f((mw - M) * LOG2E);
      l += dppf<XOR1>(l); l += dppf<XOR2>(l); l += dppf<HMIR>(l);
      const u32 goff = WS_ML + grp_off * 256;
      if (lane < 32 && (lane & 7) < 2) st_gran(ws, goff + sp * 64 + ((lane >> 3) * 2 + (lane & 1)) * 8, tag, __float_as_uint((lane & 1) ? l : M));
      lds_wait_ge<4>(ctl + C_MLTRIG, 1);
      u32x4 g;
      for (u32 spins = 0;; ++spins) {
        g = ld_gran2(rs, goff + (lane & 3) * 64 + ((lane >> 3) & 3) * 16);
        if (__all(g[1] == tag && g[3] == tag) || spins > SPIN_CAP) break;
        __builtin_amdgcn_s_sleep(8);
        PIN();
      }
      const float Mp = __uint_as_float(g[0]);
      float Ms = fmaxf(Mp, dppf<XOR1>(Mp)); Ms = fmaxf(Ms, dppf<XOR2>(Ms));
      float L = __uint_as_float(g[2]) * __builtin_amdgcn_exp2f((Mp - Ms) * LOG2E);
      L += dppf<XOR1>(L); L += dppf<XOR2>(L);
      if (lane < 32) *(LAS float*)(lds + L_FS + lane * 4) = __builtin_amdgcn_exp2f((mw - Ms) * LOG2E) / L;
    }
    bar();
    return;
  }

  {
    const int tw = hwave - NSW - 1;
    __builtin_amdgcn_s_setprio(3);
    if (tw == 2) {
      u32 pf = 0;
      {
        u64 pc; u32 back;
        asm volatile("s_getpc_b64 %0\n.Lpf_here:\n\ts_mov_b32 %1, .Lpf_here-fused_kernel" : "=s"(pc), "=s"(back));
        const u32 o = ((wg >> 3) * 8 + lane) * 128;
        if (lane < 8 && o + 4 <= CODE_BYTES) pf = *(const GAS u32*)(pc - back + o);
      }
      __builtin_amdgcn_s_sleep(TSLEEP);
      asm volatile("" ::: "memory");
      {
        u32x4 t[12];
        const int l3 = lane & 31;
#pragma unroll
        for (int c = 0; c < 4; ++c) {
          const int o = c * 64 + (c == 3 ? l3 : lane);
          t[c] = ((const u32x4*)w1)[o]; t[4 + c] = ((const u32x4*)b1)[o]; t[8 + c] = ((const u32x4*)w2)[o];
        }
#pragma unroll
        for (int c = 0; c < 12; ++c) *(LAS u32x4*)(lds + L_TW + (c >> 2) * 3584 + ((c & 3) * 64 + ((c & 3) == 3 ? l3 : lane)) * 16) = t[c];
        wait_lgkm();
        if (lane == 0) ctl[C_TW] = 1;
      }
      asm volatile("" ::"v"(pf));
      const u32x4* wb = (const u32x4*)wo + (size_t)(kvh * 512 + 256 * kh + (lane >> 3)) * 64 + 8 * ct8 + (lane & 7);
      lds_wait_ge<4>(ctl + C_WOTRIG, NSW);
      u32x4 r[32];
#pragma unroll
      for (int i = 0; i < 32; ++i) r[i] = wb[i * 512];
      SB();
      bar();
      SB();
#pragma unroll
      for (int i = 0; i < 32; ++i) *(LAS u32x4*)(lds + L_WO + i * 1024 + lane * 16) = r[i];
      wait_lgkm();
      if (lane == 0) ctl[C_WO] = 1;
      return;
    }
    lds_wait_ge<8>(ctl + C_TW, 1);
    const float b2v = bflo(b2[0]);
    u32x2 r1[7], rb[7], r2[7];
#pragma unroll
    for (int c = 0; c < 7; ++c) {
      r1[c] = *(const LAS u32x2*)(lds + L_TW + (c * 64 + lane) * 8);
      rb[c] = *(const LAS u32x2*)(lds + L_TW + 3584 + (c * 64 + lane) * 8);
      r2[c] = *(const LAS u32x2*)(lds + L_TW + 7168 + (c * 64 + lane) * 8);
    }
    const int idx0 = wg * 24 + tw * 12;
    f32x2 f1[14], fb[14], f2[14];
#pragma unroll
    for (int c = 0; c < 14; ++c) {
      f1[c] = f32x2{bflo(r1[c >> 1][c & 1]), bfhi(r1[c >> 1][c & 1])};
      fb[c] = f32x2{bflo(rb[c >> 1][c & 1]), bfhi(rb[c >> 1][c & 1])};
      f2[c] = f32x2{bflo(r2[c >> 1][c & 1]), bfhi(r2[c >> 1][c & 1])};
    }
    __builtin_amdgcn_s_setprio(0);
    float res = 0.f;
#pragma unroll 1
    for (int e = 0; e < 12; ++e) {
      const int i0 = idx0 + e;
      const float t0 = __uint_as_float((u32)((i0 >= NT_HALF ? i0 - NT_HALF + 0x8000 : i0) + (E_LO << 7)) << 16);
      const f32x2 tt = {t0, t0};
      f32x2 acc = {0.f, 0.f};
#pragma unroll
      for (int c0_ = 0; c0_ < 14; c0_ += 7) {
        f32x2 u[7], z[7];
#pragma unroll
        for (int c = 0; c < 7; ++c) u[c] = __builtin_elementwise_fma(tt, f1[c0_ + c], fb[c0_ + c]);
        SB();
#pragma unroll
        for (int c = 0; c < 7; ++c) z[c] = u[c] * u[c];
        SB();
#pragma unroll
        for (int c = 0; c < 7; ++c) z[c] = __builtin_elementwise_fma(z[c], f32x2{GK1, GK1}, f32x2{GK0, GK0});
        SB();
#pragma unroll
        for (int c = 0; c < 7; ++c) z[c] = z[c] * u[c];
        SB();
#pragma unroll
        for (int c = 0; c < 7; ++c) z[c] = f32x2{__builtin_amdgcn_exp2f(z[c][0]), __builtin_amdgcn_exp2f(z[c][1])};
        SB();
#pragma unroll
        for (int c = 0; c < 7; ++c) { z[c] = z[c] + f32x2{1.f, 1.f}; u[c] = u[c] * f2[c0_ + c]; }
        SB();
#pragma unroll
        for (int c = 0; c < 7; ++c) z[c] = f32x2{__builtin_amdgcn_rcpf(z[c][0]), __builtin_amdgcn_rcpf(z[c][1])};
        SB();
#pragma unroll
        for (int c = 0; c < 7; ++c) acc = __builtin_elementwise_fma(u[c], z[c], acc);
        SB();
      }
      const float a0 = wave_sum(acc[0] + acc[1]);
      if (lane == e) res = a0;
    }
    __builtin_amdgcn_s_setprio(3);
    lds_wait_ge<8>(ctl + C_Q, 1);
    {
      const u32 tag = ctl[C_TAG];
      const float y = rbf(res) + b2v;
      const float yn = dppf<XOR1>(y);
      if (lane < 12 && (lane & 1) == 0) st_gran(ws, WS_TB + (idx0 + lane) * 4, tag, cvtpk(y, yn));
    }
  }
}

extern "C" void kernel_launch(void* const* d_in, const int* in_sizes, int n_in, void* d_out, int out_size, void* d_ws, size_t ws_size,
                              hipStream_t stream) {
  fused_kernel<<<dim3(NWG), dim3(NTHR), 0, stream>>>((const u16*)d_in[0], (const u16*)d_in[1], (const u16*)d_in[2], (const u16*)d_in[3],
                                                     (const u16*)d_in[4], (const u16*)d_in[6], (const u16*)d_in[7], (const u16*)d_in[8],
                                                     (const u16*)d_in[9], (u16*)d_out, (unsigned char*)d_ws);
}
```
